```python
import torch

def _softmax(x, axis=-1):
    return torch.softmax(x, dim=axis)

def _gelu(x):
    return torch.nn.functional.gelu(x, approximate='tanh')

def _silu(x):
    return torch.nn.functional.silu(x)

def _sigmoid(x):
    return torch.sigmoid(x)

def _layernorm(x, axis=-1, eps=1e-05):
    m = x.mean(dim=axis, keepdim=True)
    v = x.var(dim=axis, keepdim=True, unbiased=False)
    return (x - m) / torch.sqrt(v + eps)

def _scatter_add(tgt, idx, src, axis=0):
    return tgt.clone().index_add_(axis, idx, src)

def _segment_sum(src, seg, n):
    out = torch.zeros((n,) + src.shape[1:], dtype=src.dtype)
    return out.index_add_(0, seg, src)

def _pad(x, pw):
    flat = tuple((v for pair in reversed(pw) for v in pair))
    return torch.nn.functional.pad(x, flat)
kernel_name = 'parallel_sum__linear_kernel_hedgehog__mixer_gmlp__prompt'

def setup_inputs():

    _w = lambda a: (lambda t: t.to(torch.bfloat16) if t.is_floating_point() else t)(torch.as_tensor(a))

    def _A_setup():
        B, S, D, H = (1, 2048, 576, 32)
        Dh = D // H
        return {'x': _w((lambda *s: torch.randn(*s, dtype=torch.bfloat16) * 0.05)(B, S, D)), 'wqkv': _w((lambda *s: torch.randn(*s, dtype=torch.bfloat16) * 0.4)(D, 3 * D)), 'w_phi': _w((lambda *s: torch.randn(*s, dtype=torch.bfloat16) * 0.4)(H, Dh, Dh) * Dh ** (-0.5)), 'wo': _w((lambda *s: torch.randn(*s, dtype=torch.bfloat16) * 0.4)(D, D))}
    _A_in = _A_setup()

    def _B_setup():
        B, S, D, F = (1, 2048, 576, 4608)
        Fh = F // 2
        return {'x': _w((lambda *s: torch.randn(*s, dtype=torch.bfloat16) * 0.05)(B, S, D)), 'w_in': _w((lambda *s: torch.randn(*s, dtype=torch.bfloat16) * 0.4)(D, 2 * Fh)), 'w_sp': _w(0.01 * (lambda *s: torch.randn(*s, dtype=torch.bfloat16) * 0.4)(S, S)), 'b_sp': _w(1.0 + 0.01 * (lambda *s: torch.randn(*s, dtype=torch.bfloat16) * 0.05)(S)), 'w_out': _w((lambda *s: torch.randn(*s, dtype=torch.bfloat16) * 0.05)(Fh, D))}
    _B_in = _B_setup()
    return {'A_x': _A_in['x'], 'A_wqkv': _A_in['wqkv'], 'A_w_phi': _A_in['w_phi'], 'A_wo': _A_in['wo'], 'B_x': _B_in['x'], 'B_w_in': _B_in['w_in'], 'B_w_sp': _B_in['w_sp'], 'B_b_sp': _B_in['b_sp'], 'B_w_out': _B_in['w_out']}

def reference(A_x, A_wqkv, A_w_phi, A_wo, B_x, B_w_in, B_w_sp, B_b_sp, B_w_out):

    def _A_ref(x, wqkv, w_phi, wo):
        B, S, D = x.shape
        H = 32
        Dh = D // H
        qkv = (lambda x, *d: x.permute(*d))((x @ wqkv).reshape(B, S, 3, H, Dh), 2, 0, 3, 1, 4)
        q, k, v = (qkv[0], qkv[1], qkv[2])
        pq = torch.einsum('bhsd,hdf->bhsf', q, w_phi)
        pk = torch.einsum('bhsd,hdf->bhsf', k, w_phi)
        phi_q = torch.concatenate([_softmax(pq, axis=-1), _softmax(-pq, axis=-1)], axis=-1)
        phi_k = torch.concatenate([_softmax(pk, axis=-1), _softmax(-pk, axis=-1)], axis=-1)
        kv = torch.einsum('bhsf,bhsd->bhfd', phi_k, v)
        z = torch.einsum('bhsf->bhf', phi_k)
        num = torch.einsum('bhqf,bhfd->bhqd', phi_q, kv)
        den = torch.einsum('bhqf,bhf->bhq', phi_q, z)[..., None] + 1e-06
        o = num / den
        return (lambda x, *d: x.permute(*d))(o, 0, 2, 1, 3).reshape(B, S, D) @ wo

    def _B_ref(x, w_in, w_sp, b_sp, w_out):
        B, S, D = x.shape
        F = w_in.shape[-1]
        Fh = F // 2
        z = _gelu(torch.einsum('bsd,df->bsf', x, w_in))
        z1 = z[..., :Fh].clone()
        z2 = _layernorm(z[..., Fh:], axis=-1)
        g = torch.einsum('ts,bsf->btf', w_sp, z2) + b_sp[None, :, None]
        return torch.einsum('bsf,fd->bsd', z1 * g, w_out)
    x = A_x
    xn = _layernorm(x, axis=-1)
    return x + _A_ref(xn, A_wqkv, A_w_phi, A_wo) + _B_ref(xn, B_w_in, B_w_sp, B_b_sp, B_w_out)
```

```cpp
#include <hip/hip_runtime.h>
#include <hip/hip_bf16.h>
typedef unsigned short u16;
__device__ __forceinline__ float b2f(u16 v){ return __uint_as_float(((unsigned)v)<<16); }
__device__ __forceinline__ u16 f2b(float f){ __hip_bfloat16 h = __float2bfloat16(f); return *(u16*)&h; }
constexpr int S=2048, D=576, H=32, Dh=18, F=4608, Fh=2304;

__global__ void ln_k(const u16* in, int ld, int n, u16* out){
  int r = blockIdx.x; const u16* p = in + (size_t)r*ld;
  __shared__ float red[256];
  float s=0; for(int i=threadIdx.x;i<n;i+=256) s+=b2f(p[i]);
  red[threadIdx.x]=s; __syncthreads();
  for(int o=128;o>0;o>>=1){ if(threadIdx.x<o) red[threadIdx.x]+=red[threadIdx.x+o]; __syncthreads(); }
  float m=red[0]/n; __syncthreads();
  s=0; for(int i=threadIdx.x;i<n;i+=256){ float d=b2f(p[i])-m; s+=d*d; }
  red[threadIdx.x]=s; __syncthreads();
  for(int o=128;o>0;o>>=1){ if(threadIdx.x<o) red[threadIdx.x]+=red[threadIdx.x+o]; __syncthreads(); }
  float rs=rsqrtf(red[0]/n+1e-5f);
  for(int i=threadIdx.x;i<n;i+=256) out[(size_t)r*n+i]=f2b((b2f(p[i])-m)*rs);
}
__global__ void gemm_k(const u16* A,int lda,const u16* B,int ldb,int K,int N,int mode,u16* C,float* Cf,const u16* aux,int ldaux,const u16* bias){
  __shared__ float As[64][17], Bs[16][65];
  int bm=blockIdx.y*64, bn=blockIdx.x*64;
  int tx=threadIdx.x&15, ty=threadIdx.x>>4;
  float acc[4][4]={};
  for(int k0=0;k0<K;k0+=16){
    for(int i=threadIdx.x;i<1024;i+=256){ int r=i>>4,c=i&15; As[r][c]=b2f(A[(size_t)(bm+r)*lda+k0+c]); }
    for(int i=threadIdx.x;i<1024;i+=256){ int r=i>>6,c=i&63; Bs[r][c]=b2f(B[(size_t)(k0+r)*ldb+bn+c]); }
    __syncthreads();
    for(int k=0;k<16;k++){
      float a[4],b[4];
      for(int i=0;i<4;i++) a[i]=As[ty*4+i][k];
      for(int j=0;j<4;j++) b[j]=Bs[k][tx*4+j];
      for(int i=0;i<4;i++)for(int j=0;j<4;j++) acc[i][j]+=a[i]*b[j];
    }
    __syncthreads();
  }
  for(int i=0;i<4;i++)for(int j=0;j<4;j++){
    int r=bm+ty*4+i,c=bn+tx*4+j; float v=acc[i][j];
    if(mode==0) C[(size_t)r*N+c]=f2b(v);
    else if(mode==1){ float t=0.7978845608f*(v+0.044715f*v*v*v); C[(size_t)r*N+c]=f2b(0.5f*v*(1.f+tanhf(t))); }
    else if(mode==2){ C[(size_t)r*N+c]=f2b(b2f(aux[(size_t)r*ldaux+c])*(v+b2f(bias[r]))); }
    else if(mode==3){ Cf[(size_t)r*N+c]=v; }
    else { C[(size_t)r*N+c]=f2b(b2f(aux[(size_t)r*ldaux+c])+Cf[(size_t)r*N+c]+v); }
  }
}
__global__ void phi_k(const u16* qkv,const u16* wphi,float* phi){
  int idx=blockIdx.x*256+threadIdx.x; int t=idx%S; int h=(idx/S)%H; int w=idx/(S*H);
  float p[Dh];
  #pragma unroll
  for(int f=0;f<Dh;f++) p[f]=0;
  #pragma unroll
  for(int d=0;d<Dh;d++){ float q=b2f(qkv[(size_t)t*3*D+w*D+h*Dh+d]);
    #pragma unroll
    for(int f=0;f<Dh;f++) p[f]+=q*b2f(wphi[(h*Dh+d)*Dh+f]); }
  float mx=-1e30f,mn=1e30f;
  #pragma unroll
  for(int f=0;f<Dh;f++){ mx=fmaxf(mx,p[f]); mn=fminf(mn,p[f]); }
  float s0=0,s1=0;
  #pragma unroll
  for(int f=0;f<Dh;f++){ s0+=__expf(p[f]-mx); s1+=__expf(mn-p[f]); }
  float* o=phi+(((size_t)w*H+h)*S+t)*36;
  #pragma unroll
  for(int f=0;f<Dh;f++){ o[f]=__expf(p[f]-mx)/s0; o[18+f]=__expf(mn-p[f])/s1; }
}
__global__ void kv_k(const float* phi,const u16* qkv,float* kv){
  int h=blockIdx.x; int i=threadIdx.x; if(i>=36*19) return; int f=i/19,d=i%19;
  const float* pk=phi+((size_t)H+h)*S*36; float s=0;
  for(int t=0;t<S;t++){ float v= d<18? b2f(qkv[(size_t)t*3*D+2*D+h*Dh+d]) : 1.f; s+=pk[t*36+f]*v; }
  kv[(h*36+f)*19+d]=s;
}
__global__ void o_k(const float* phi,const float* kv,u16* o){
  int idx=blockIdx.x*256+threadIdx.x; int c=idx%D; int t=idx/D; int h=c/Dh,d=c%Dh;
  const float* pq=phi+((size_t)h*S+t)*36; float n=0,de=0;
  for(int f=0;f<36;f++){ n+=pq[f]*kv[(h*36+f)*19+d]; de+=pq[f]*kv[(h*36+f)*19+18]; }
  o[idx]=f2b(n/(de+1e-6f));
}
extern "C" void kernel_launch(void* const* d_in, const int* in_sizes, int n_in, void* d_out, int out_size, void* d_ws, size_t ws_size, hipStream_t stream){
  const u16* x=(const u16*)d_in[0]; const u16* wqkv=(const u16*)d_in[1]; const u16* wphi=(const u16*)d_in[2]; const u16* wo=(const u16*)d_in[3];
  const u16* win=(const u16*)d_in[5]; const u16* wsp=(const u16*)d_in[6]; const u16* bsp=(const u16*)d_in[7]; const u16* wout=(const u16*)d_in[8];
  char* ws=(char*)d_ws; size_t off=0; auto al=[&](size_t b){ char* p=ws+off; off+=(b+255)&~255ull; return p; };
  u16* xn=(u16*)al((size_t)S*D*2); u16* qkv=(u16*)al((size_t)S*3*D*2); u16* z=(u16*)al((size_t)S*F*2); u16* z2=(u16*)al((size_t)S*Fh*2);
  u16* u=(u16*)al((size_t)S*Fh*2); float* phi=(float*)al((size_t)2*H*S*36*4); float* kv=(float*)al(H*36*19*4); u16* o=(u16*)al((size_t)S*D*2); float* ao=(float*)al((size_t)S*D*4);
  ln_k<<<S,256,0,stream>>>(x,D,D,xn);
  gemm_k<<<dim3(3*D/64,S/64),256,0,stream>>>(xn,D,wqkv,3*D,D,3*D,0,qkv,nullptr,nullptr,0,nullptr);
  gemm_k<<<dim3(F/64,S/64),256,0,stream>>>(xn,D,win,F,D,F,1,z,nullptr,nullptr,0,nullptr);
  ln_k<<<S,256,0,stream>>>(z+Fh,F,Fh,z2);
  gemm_k<<<dim3(Fh/64,S/64),256,0,stream>>>(wsp,S,z2,Fh,S,Fh,2,u,nullptr,z,F,bsp);
  phi_k<<<2*H*S/256,256,0,stream>>>(qkv,wphi,phi);
  kv_k<<<H,704,0,stream>>>(phi,qkv,kv);
  o_k<<<S*D/256,256,0,stream>>>(phi,kv,o);
  gemm_k<<<dim3(D/64,S/64),256,0,stream>>>(o,D,wo,D,D,D,3,nullptr,ao,nullptr,0,nullptr);
  gemm_k<<<dim3(D/64,S/64),256,0,stream>>>(u,Fh,wout,D,Fh,D,4,(u16*)d_out,ao,x,D,nullptr);
}
```

```cpp
#include <hip/hip_runtime.h>
#include <type_traits>

typedef unsigned short u16;
typedef unsigned long long u64;
typedef __attribute__((ext_vector_type(8))) __bf16 bf16x8;
typedef __attribute__((ext_vector_type(4))) __bf16 bf16x4;
typedef __attribute__((ext_vector_type(2))) __bf16 bf16x2;
typedef __attribute__((ext_vector_type(4))) short s16x4;
typedef __attribute__((ext_vector_type(2))) float f32x2;
typedef __attribute__((ext_vector_type(4))) float f32x4;
typedef __attribute__((ext_vector_type(16))) float f32x16;
typedef __attribute__((ext_vector_type(2))) unsigned u32x2;
typedef __attribute__((ext_vector_type(4))) unsigned u32x4;
#define GAS __attribute__((address_space(1)))
#define LAS __attribute__((address_space(3)))
#define DI __device__ __forceinline__
#define SCHED() __builtin_amdgcn_sched_barrier(0)
#define BAR() do { asm volatile("" ::: "memory"); __builtin_amdgcn_s_barrier(); asm volatile("" ::: "memory"); } while (0)
#define LGKM0() __builtin_amdgcn_s_waitcnt(0xC07F)
#define VMW(n) asm volatile("s_waitcnt vmcnt(%0)" ::"i"((n) > 63 ? 63 : (n)) : "memory")
#define MFMA16(a, b, c) __builtin_amdgcn_mfma_f32_16x16x32_bf16((a), (b), (c), 0, 0, 0)
#define MFMA32(a, b, c) __builtin_amdgcn_mfma_f32_32x32x16_bf16((a), (b), (c), 0, 0, 0)

template <int B, int E, class F> DI void sfor(F&& f) {
  if constexpr (B < E) { f(std::integral_constant<int, B>{}); sfor<B + 1, E>(f); }
}

constexpr unsigned WS_EPOCH = 0;
constexpr unsigned WS_FZ2 = 4096;
constexpr unsigned WS_FKVP = WS_FZ2 + 8192;
constexpr unsigned WS_FKVB = WS_FKVP + 2048;
constexpr unsigned WS_FO = WS_FKVB + 2048;
constexpr unsigned WS_FU = WS_FO + 4096;
constexpr unsigned WS_STAT = 65536;
constexpr unsigned WS_KVP = WS_STAT + 524288;
constexpr unsigned WS_KVB = WS_KVP + 256 * 6144;
constexpr unsigned WS_OL = WS_KVB + 16 * 3072;
constexpr unsigned WS_OG = WS_OL + 16 * 16384;
constexpr unsigned WS_W3 = WS_OG + 128 * 16384;
constexpr unsigned WS_Z2 = WS_W3 + 8 * 45 * 9216;
constexpr unsigned WS_U = WS_Z2 + 16 * 64 * 9216;
constexpr unsigned WS_DBG = WS_U + 16 * 4 * 16 * 9216;
constexpr unsigned WS_END = WS_DBG + 256 * 8 * 32 * 8;

constexpr int SL1 = 18432;
constexpr int SL2 = 34816;
constexpr int SL3 = 36864;
constexpr int LDS_Z = 126976;
constexpr int LDS_Q = LDS_Z;
constexpr int LDS_K = 143360;
constexpr int LDS_V = 153600;
constexpr int LDS_TOTAL = 163840;

constexpr int NB_XDONE = 10, NB_A0 = 11, NB_ALAST = 19, NB_ADONE = 20, NB_B0 = 21, NB_BLAST = 29, NB_BDONE = 30, NB_C0 = 31,
              NB_CDONE = 40, NB_G2 = 41  , NB_G2DONE = 73;

#define STAMP(k) do {} while (0)

template <int OFF, int SC1> DI void glds_m0(const char* sbase, unsigned voff, unsigned ldsaddr) {
  if constexpr (SC1) asm volatile("s_mov_b32 m0, %2\n\ts_nop 0\n\tglobal_load_lds_dwordx4 %0, %1 offset:%3 sc1" ::"v"(voff), "s"(sbase), "s"(ldsaddr), "i"(OFF) : "memory");
  else asm volatile("s_mov_b32 m0, %2\n\ts_nop 0\n\tglobal_load_lds_dwordx4 %0, %1 offset:%3" ::"v"(voff), "s"(sbase), "s"(ldsaddr), "i"(OFF) : "memory");
}
template <int OFF, int SC1> DI void glds(const char* sbase, unsigned voff) {
  if constexpr (SC1) asm volatile("global_load_lds_dwordx4 %0, %1 offset:%2 sc1" ::"v"(voff), "s"(sbase), "i"(OFF) : "memory");
  else asm volatile("global_load_lds_dwordx4 %0, %1 offset:%2" ::"v"(voff), "s"(sbase), "i"(OFF) : "memory");
}
template <int NP> DI void issue_tile(const char* sbase, const unsigned (&v)[NP], unsigned lds) {
  sfor<0, NP>([&](auto P) {
    constexpr int p = P;
    if constexpr ((p & 3) == 0) glds_m0<0, 0>(sbase, v[p], lds + p * 1024);
    else glds<(p & 3) * 1024, 0>(sbase, v[p]);
  });
}
template <int NP> DI void issue_lin(const char* sbase, unsigned vl, unsigned lds) {
  sfor<0, NP>([&](auto P) {
    constexpr int p = P;
    if constexpr ((p & 3) == 0) glds_m0<0, 1>(sbase + p * 1024, vl, lds + p * 1024);
    else glds<(p & 3) * 1024, 1>(sbase + (p & ~3) * 1024, vl);
  });
}
DI bf16x8 lds_b128(const LAS char* p) { return *(const LAS bf16x8*)p; }
DI bf16x8 lds_tr(const LAS char* p0, const LAS char* p1) {
  bf16x4 a = __builtin_amdgcn_ds_read_tr16_b64_v4bf16((LAS bf16x4*)p0);
  bf16x4 b = __builtin_amdgcn_ds_read_tr16_b64_v4bf16((LAS bf16x4*)p1);
  return __builtin_shufflevector(a, b, 0, 1, 2, 3, 4, 5, 6, 7);
}
DI unsigned pk2(float a, float b) { f32x2 v = {a, b}; return __builtin_bit_cast(unsigned, __builtin_convertvector(v, bf16x2)); }
DI float blo(unsigned u) { return __uint_as_float(u << 16); }
DI float bhi(unsigned u) { return __uint_as_float(u & 0xffff0000u); }
template <int CTRL> DI float dpp_add(float x) { return x + __int_as_float(__builtin_amdgcn_update_dpp(0, __float_as_int(x), CTRL, 0xf, 0xf, true)); }
DI float row_sum16(float x) { x = dpp_add<0x128>(x); x = dpp_add<0x124>(x); x = dpp_add<0x122>(x); return dpp_add<0x121>(x); }
DI float quad_rows_sum(float x) {
  auto a = __builtin_amdgcn_permlane32_swap(__float_as_uint(x), __float_as_uint(x), false, false);
  x = __uint_as_float(a[0]) + __uint_as_float(a[1]);
  auto b = __builtin_amdgcn_permlane16_swap(__float_as_uint(x), __float_as_uint(x), false, false);
  return __uint_as_float(b[0]) + __uint_as_float(b[1]);
}
DI f32x2 gelu2(f32x2 x) {
  const float k0 = -2.302208198f, k1 = -0.1029432397f;
  f32x2 t = x * ((x * x) * k1 + k0);
  f32x2 d;
  d.x = __builtin_amdgcn_exp2f(t.x); d.y = __builtin_amdgcn_exp2f(t.y);
  d = d + 1.0f;
  f32x2 r;
  r.x = __builtin_amdgcn_rcpf(d.x); r.y = __builtin_amdgcn_rcpf(d.y);
  return x * r;
}
DI int lane_id() { int l; asm volatile("v_mbcnt_lo_u32_b32 %0, -1, 0\n\tv_mbcnt_hi_u32_b32 %0, -1, %0" : "=v"(l)); return l; }
template <class T> DI void pin(T& x) { asm volatile("" : "+v"(x)); }
typedef __amdgpu_buffer_rsrc_t rsrc_t;
DI void st16(rsrc_t r, unsigned off, u32x4 v) { __builtin_amdgcn_raw_buffer_store_b128(v, r, off, 0, 16); }
DI void st8(rsrc_t r, unsigned off, u32x2 v) { __builtin_amdgcn_raw_buffer_store_b64(v, r, off, 0, 16); }
DI void st4(rsrc_t r, unsigned off, unsigned v) { __builtin_amdgcn_raw_buffer_store_b32(v, r, off, 0, 16); }
DI u32x4 ld16(rsrc_t r, unsigned off) { return __builtin_amdgcn_raw_buffer_load_b128(r, off, 0, 16); }
DI u32x4 ld16s(rsrc_t r, unsigned off, unsigned soff) { return __builtin_amdgcn_raw_buffer_load_b128(r, off, soff, 16); }
DI u32x2 ld8(rsrc_t r, unsigned off) { return __builtin_amdgcn_raw_buffer_load_b64(r, off, 0, 16); }
DI u64 ld_flag(char* ws, unsigned off) { return __hip_atomic_load((GAS u64*)(ws + off), __ATOMIC_RELAXED, __HIP_MEMORY_SCOPE_AGENT); }
DI void st_flag(char* ws, unsigned off, u64 tag) { __hip_atomic_store((GAS u64*)(ws + off), tag, __ATOMIC_RELAXED, __HIP_MEMORY_SCOPE_AGENT); }
constexpr int SPIN_CAP = 1 << 15;
DI void wait_flags(char* ws, unsigned off, u64 tag) {
#pragma nounroll
  for (int it = 0; it < SPIN_CAP; ++it) {
    if (__all(ld_flag(ws, off) == tag)) break;
    __builtin_amdgcn_s_sleep(2);
  }
  asm volatile("" ::: "memory");
}

struct Params {
  const u16 *x, *wqkv, *wphi, *wo, *win, *wsp, *bsp, *wout;
  u16* out;
  char* ws;
};

template <bool FIRST> DI void mfma_t(f32x4& d, const bf16x8& a, const bf16x8& b) {
  if constexpr (FIRST) asm volatile("v_mfma_f32_16x16x32_bf16 %0, %1, %2, 0" : "=&v"(d) : "v"(a), "v"(b));
  else asm volatile("v_mfma_f32_16x16x32_bf16 %0, %1, %2, %0" : "+v"(d) : "v"(a), "v"(b));
}
DI bf16x4 tr4(const LAS char* p) { return __builtin_amdgcn_ds_read_tr16_b64_v4bf16((LAS bf16x4*)p); }
template <int KIND, int NBLK, int SLOT0> DI void gemm_pass(const bf16x8 (&xf)[2][18], f32x4 (&acc)[9][2], const LAS char* smem) {
  constexpr int PD = 4, NF = 18 * NBLK;
  const int l = lane_id();
  unsigned a0[2], a6[2];
  a0[0] = (l >> 2) * 288 + (l & 3) * 8; a0[1] = a0[0] + 3 * SL1; pin(a0[0]); pin(a0[1]);
  if constexpr (KIND == 1) { a6[0] = (l >> 2) * 288 + 72 + 80 * ((l & 3) == 3 ? 2 : (l & 3)); a6[1] = a6[0] + 3 * SL1; pin(a6[0]); pin(a6[1]); }
  bf16x4 fr[PD + 1][2];
  auto rd = [&](auto N, auto H) {
    constexpr int n = N, hf = H, kt = n / (2 * NBLK), ks = (n / NBLK) & 1, f = n % NBLK;
    constexpr int sl = (SLOT0 + kt) % 6, hi = sl / 3, base = (sl % 3) * SL1 + ks * 9216 + hf * 4608;
    if constexpr (n % (2 * NBLK) == 0 && hf == 0) { BAR(); SCHED(); }
    if constexpr (KIND == 1 && f == 6) fr[n % (PD + 1)][hf] = tr4(smem + a6[hi] + base);
    else fr[n % (PD + 1)][hf] = tr4(smem + a0[hi] + base + (KIND == 1 ? (f >> 1) * 80 + (f & 1) * 32 : f * 32));
    SCHED();
  };
  const std::integral_constant<int, 0> I0; const std::integral_constant<int, 1> I1;
  SCHED();
  sfor<0, PD>([&](auto N) { rd(N, I0); rd(N, I1); });
  sfor<0, NF>([&](auto N) {
    constexpr int n = N, s = n / NBLK, f = n % NBLK, b0 = f & 1, b1 = b0 ^ 1;
    const bf16x8 wv = __builtin_shufflevector(fr[n % (PD + 1)][0], fr[n % (PD + 1)][1], 0, 1, 2, 3, 4, 5, 6, 7);
    if constexpr (KIND == 0) mfma_t<s == 0>(acc[f][b0], xf[b0][s], wv); else mfma_t<s == 0>(acc[f][b0], wv, xf[b0][s]);
    SCHED();
    if constexpr (n + PD < NF) rd(std::integral_constant<int, n + PD>{}, I0);
    if constexpr (KIND == 0) mfma_t<s == 0>(acc[f][b1], xf[b1][s], wv); else mfma_t<s == 0>(acc[f][b1], wv, xf[b1][s]);
    SCHED();
    if constexpr (n + PD < NF) rd(std::integral_constant<int, n + PD>{}, I1);
  });
  BAR();
  asm volatile("s_nop 7" ::: "memory");
  SCHED();
}

template <class T> DI void hedgehog(const f32x16& p0, const f32x16& p1, u32x4 (&ph)[5], T&& tick) {
  float p[18];
  sfor<0, 16>([&](auto I) { p[I] = p0[(int)I]; });
  p[16] = p1[0]; p[17] = p1[1];
  float mx = p[0], mn = p[0];
  sfor<1, 18>([&](auto I) { mx = fmaxf(mx, p[I]); mn = fminf(mn, p[I]); });
  const float L2E = 1.4426950408889634f;
  const float cx = -mx * L2E, cn = mn * L2E;
  tick();
  float e[36], s0 = 0.f, s1 = 0.f;
  sfor<0, 18>([&](auto I) { e[I] = __builtin_amdgcn_exp2f(__builtin_fmaf(p[I], L2E, cx)); s0 += e[I]; if constexpr (I == 11) tick(); });
  sfor<0, 18>([&](auto I) { e[18 + I] = __builtin_amdgcn_exp2f(__builtin_fmaf(p[I], -L2E, cn)); s1 += e[18 + I]; if constexpr (I == 5) tick(); });
  tick();
  const float r0 = __builtin_amdgcn_rcpf(s0), r1 = __builtin_amdgcn_rcpf(s1);
  sfor<0, 18>([&](auto I) { constexpr int i = I; const float r = i < 9 ? r0 : r1; ph[i >> 2][i & 3] = pk2(e[2 * i] * r, e[2 * i + 1] * r); });
  ph[4][2] = 0; ph[4][3] = 0;
  sfor<0, 5>([&](auto I) { pin(ph[I]); });
  tick();
}

__global__ __launch_bounds__(512, 2) void fused_block(Params P) {
  extern __shared__ __attribute__((aligned(1024))) char smem_g[];
  const LAS char* smem = (const LAS char*)smem_g;
  LAS char* smw = (LAS char*)smem_g;
  const unsigned lds0 = (unsigned)(size_t)smem;
  char* ws = P.ws;
  const int bid = blockIdx.x, xcd = bid & 7, rr = bid >> 3;
  const int i = (xcd >> 1) * 4 + (rr >> 3), j = (xcd & 1) * 8 + (rr & 7);
  const int w = __builtin_amdgcn_readfirstlane(threadIdx.x >> 6), l = threadIdx.x & 63;
  const int c = l & 15, q = l >> 4;
  const int rot = (2 * (i & 3) + (i >> 2) + j) % 9;
  const rsrc_t rw = __builtin_amdgcn_make_buffer_rsrc(ws, 0, WS_END, 0x00020000);
  STAMP(0);

  if (w == 4) {
    unsigned vx[16], vw[18];
    sfor<0, 16>([&](auto Pp) { constexpr int p = Pp; const int row = p * 8 + (l >> 3), cc = row & 15;
      const int tok = (row & ~31) + 8 * (cc >> 2) + 4 * ((row >> 4) & 1) + (cc & 3);
      vx[p] = tok * 1152 + (((l & 7) ^ ((cc >> 1) & 7)) << 4) + 4096 - (p & 3) * 1024; });
    const char* xb = (const char*)P.x + (size_t)i * 128 * 1152 - 4096;
    auto ktile = [&](int n) { int k = n + rot; return k >= 9 ? k - 9 : k; };
    auto issue_x = [&](int n) { issue_tile<16>(xb + ktile(n) * 128, vx, lds0 + (n % 6) * SL1); };
    issue_x(0); issue_x(1); issue_x(2);
    const u64 tag = *(const u64*)(ws + WS_EPOCH) + 1;
    auto krow = [](int pos) { return (pos & 32) + 8 * ((pos >> 2) & 3) + 4 * ((pos >> 4) & 1) + (pos & 3); };
    sfor<0, 18>([&](auto Pp) { constexpr int p = Pp; const int n = p * 64 + l, pos = n / 18, m = n - pos * 18;
      int mb = (m >> 1) + (j & 1); if (mb == 9) mb = 0;
      vw[p] = krow(pos) * 9216 + mb * 32 + (m & 1) * 16 + 4096 - (p & 3) * 1024; });
    const char* wz1 = (const char*)P.win + j * 288 - 4096;
    auto issue_w = [&](int n, int half) { issue_tile<18>(wz1 + half * 4608 + ktile(n % 9) * (64 * 9216), vw, lds0 + (n % 6) * SL1); };
    LGKM0();
    sfor<0, 9>([&](auto N) { constexpr int n = N;
      if constexpr (n + 3 < 9) { issue_x(n + 3); VMW(48); }
      else { issue_w(n + 3, 1); VMW((8 - n) * 16 + (n - 5) * 18); }
      BAR(); });
    BAR();
    issue_w(12, 1); issue_w(13, 1); issue_w(14, 1);
    unsigned vq[18];
    sfor<0, 18>([&](auto Pp) { constexpr int p = Pp; const int n = p * 64 + l, pos = n / 18; int m = n - pos * 18; if (m > 14) m = 14;
      const int sec = m / 5, mm = m - sec * 5;
      vq[p] = krow(pos) * 3456 + sec * 1152 + (mm < 4 ? mm * 16 : 56) + 4096 - (p & 3) * 1024; });
    const char* wq = (const char*)P.wqkv + j * 72 - 4096;
    auto issue_n = [&](auto N) { constexpr int n = N;
      if constexpr (n < 18) issue_w(n, 1);
      else if constexpr (n < 27) issue_tile<18>(wq + ktile(n % 9) * (64 * 3456), vq, lds0 + (n % 6) * SL1);
      else if constexpr (n < 36) issue_w(n, 0); };
    sfor<0, 3>([&](auto S) { constexpr int first = 9 + 9 * S;
      sfor<0, 9>([&](auto K) { constexpr int m = first + K;
        constexpr int hi0 = (m + 3 > first + 5 ? m + 3 : first + 5), hi = hi0 > 35 ? 35 : hi0;
        VMW((hi - m) * 18);
        BAR();
        if constexpr (m + 4 > first + 5 && m + 4 < 36) issue_n(std::integral_constant<int, m + 4>{}); });
      BAR();
      if constexpr (first + 13 < 36) { issue_n(std::integral_constant<int, first + 13>{}); issue_n(std::integral_constant<int, first + 14>{}); } });
    STAMP(1);
    unsigned va[16];
    sfor<0, 16>([&](auto Pp) { constexpr int p = Pp; const int row = p * 8 + (l >> 3), cc = row & 15;
      const int tok = (row & ~31) + 8 * (cc >> 2) + 4 * ((row >> 4) & 1) + (cc & 3);
      va[p] = tok * 4096 + (((l & 7) ^ ((cc >> 1) & 7)) << 4) + 4096 - (p & 3) * 1024; });
    const unsigned vl = l * 16;
    const char* ab = (const char*)P.wsp + (size_t)i * 128 * 4096 - 4096;
    const char* zb = ws + WS_Z2 + j * (64 * 9216);
    const int ph = (i + j) & 3;
    auto issue_g2 = [&](int s) { const int t = ((s & 3) == ph) ? s : ((s - 4) & 31); const unsigned sl = lds0 + (s & 3) * SL2;
      issue_tile<16>(ab + t * 128, va, sl); issue_lin<18>(zb + t * 18432, vl, sl + 16384); };
    wait_flags(ws, WS_FZ2 + j * 512 + ((l & 3) == 3 ? l - 1 : l) * 8, tag);
    STAMP(2);
    issue_g2(0); issue_g2(1);
    VMW(34); BAR();
    issue_g2(2); issue_g2(3);
    VMW(63); BAR();
    for (int t = 2; t < 30; ++t) { issue_g2(t + 2); VMW(63); BAR(); }
    wait_flags(ws, WS_FO + i * 256 + (l & 31) * 8, tag);
    if (bid == 0 && l == 0) st_flag(ws, WS_EPOCH, tag);
    VMW(0);
    STAMP(3);
    const char* w3 = ws + WS_W3 + (j >> 1) * (45 * 9216);
    auto issue_g3 = [&](int r) { const unsigned sl = lds0 + (r & 3) * SL3;
      sfor<0, 4>([&](auto W) { constexpr int ww = W; issue_lin<9>(w3 + (ww * 9 + r) * 9216, vl, sl + ww * 9216); }); };
    BAR();
    issue_g3(0);
    BAR();
    issue_g3(1);
    BAR();
    issue_g3(2); issue_g3(3);
    VMW(63); BAR();
    for (int r = 1; r < 6; ++r) { BAR(); issue_g3(r + 3); VMW(63); }
    BAR();
    VMW(36); BAR();
    VMW(0); BAR();
    STAMP(4);
  } else if (w > 4) {
    const int m = w - 5;
    int nb = 0, lim = 0;
    auto tick = [&]() { SCHED(); asm volatile("s_cmp_ge_i32 %0, %1\n\ts_cbranch_scc1 1f\n\ts_barrier\n\ts_add_i32 %0, %0, 1\n1:" : "+s"(nb) : "s"(lim) : "scc", "memory"); SCHED(); };
    auto upto = [&](int n) { SCHED(); asm volatile("0:\n\ts_cmp_ge_i32 %0, %1\n\ts_cbranch_scc1 1f\n\ts_barrier\n\ts_add_i32 %0, %0, 1\n\ts_branch 0b\n1:" : "+s"(nb) : "s"(n) : "scc", "memory"); SCHED(); };
    const u64 tag = *(const u64*)(ws + WS_EPOCH) + 1;
    auto wait_tick = [&](unsigned off) {
#pragma nounroll
      for (int it = 0; it < SPIN_CAP; ++it) { if (__all(ld_flag(ws, off) == tag)) break; tick(); __builtin_amdgcn_s_sleep(1); }
      asm volatile("" ::: "memory"); };
    const int t32 = l & 31, h = l >> 5;
    bf16x8 wf[2][3];
    { const int hp = (t32 >> 2) & 1, reg = (t32 & 3) + 4 * (t32 >> 3);
      const u16* wp = P.wphi + (2 * j + hp) * 324;
      sfor<0, 6>([&](auto I) { constexpr int bk = I / 3, s = I % 3; u32x4 v;
        sfor<0, 4>([&](auto E) { constexpr int e = E; unsigned x[2];
          sfor<0, 2>([&](auto O) { const int d = 16 * s + 8 * h + 2 * e + O - 18 * hp, f = 16 * bk + reg;
            const bool ok = d >= 0 && d < 18 && f < 18; const unsigned val = wp[ok ? d * 18 + f : 0]; x[O] = ok ? val : 0u; });
          v[e] = x[0] | (x[1] << 16); });
        pin(v); wf[bk][s] = __builtin_bit_cast(bf16x8, v); }); }
    LGKM0();
    upto(NB_XDONE);
    if (bid < 180) {
      const int Pq = bid >> 2, Qr = bid & 3;
      const bool isw = Pq < 36;
      const char* src = (const char*)(isw ? P.wout : P.wo) + Qr * 288;
      sfor<0, 6>([&](auto Pp) { const int p = m * 6 + Pp, n = p * 64 + l, pos = n / 18, mm = n - pos * 18;
        int row;
        if (isw) row = 64 * Pq + pos;
        else { const int so = 2 * (Pq - 36) + (pos >> 5), qq = (pos >> 2) & 3, jj = 4 * ((pos >> 4) & 1) + (pos & 3);
          row = so < 16 ? 36 * so + 18 * (qq >> 1) + 8 * (qq & 1) + jj : 36 * (8 * (so - 16) + 2 * qq + (jj >> 2)) + 18 * ((jj >> 1) & 1) + 16 + (jj & 1); }
        glds_m0<0, 0>(src, row * 1152 + mm * 16, lds0 + LDS_Z + p * 1024); });
      VMW(0);
    }
    upto(NB_A0);
    if (bid < 180) {
      const int Pq = bid >> 2, Qr = bid & 3;
      sfor<0, 6>([&](auto Pp) { const int fi = m * 6 + Pp, cgp = fi >= 9, x = fi - 9 * cgp;
        const int p = l & 3;
        const unsigned a = LDS_Z + (l >> 2) * 288 + 144 * cgp + (x < 8 ? (x >> 2) * 9216 + (x & 3) * 32 + 8 * p : (p >> 1) * 9216 + 128 + 8 * (p & 1));
        const bf16x8 fr = lds_tr(smem + a, smem + a + 4608);
        st16(rw, WS_W3 + ((2 * Qr + cgp) * 45 + Pq) * 9216 + x * 1024 + l * 16, __builtin_bit_cast(u32x4, fr)); });
    }
    STAMP(1);
    upto(NB_B0);
    {
      u32x4 zr[12];
      sfor<0, 12>([&](auto I) { constexpr int ii = I; const int fo = ii < 9 ? m * 9 + ii : 27 + 3 * m + (ii - 9);
        zr[ii] = *(const LAS u32x4*)(smem + LDS_Z + fo * 1024 + l * 16); });
      LGKM0();
      lim = NB_CDONE - 1;
      const unsigned tlo = (unsigned)tag, thi = (unsigned)(tag >> 32);
      sfor<0, 2>([&](auto G) { const int g = G ? 3 : m;
        const unsigned so = WS_STAT + ((i * 16 + c) * 128 + g * 32 + q * 8) * 16;
        u32x4 gr[8];
#pragma nounroll
        for (int it = 0; it < SPIN_CAP; ++it) {
          bool ok = true;
          sfor<0, 8>([&](auto E) { gr[E] = ld16(rw, so + E * 16); });
          sfor<0, 8>([&](auto E) { ok = ok & (gr[E].y == tlo) & (gr[E].w == thi); });
          if (__all(ok)) break;
          tick(); __builtin_amdgcn_s_sleep(1);
        }
        if constexpr (G == 0) STAMP(2);
        tick();
        float sc[8], of[8];
        sfor<0, 8>([&](auto E) {
          const float mu = row_sum16(__uint_as_float(gr[E].x)) * (1.f / 2304.f), e2 = row_sum16(__uint_as_float(gr[E].z)) * (1.f / 2304.f);
          sc[E] = __builtin_amdgcn_rsqf(fmaxf(e2 - mu * mu, 0.f) + 1e-5f); of[E] = -mu * sc[E];
          if constexpr (E == 3 || E == 7) tick(); });
        sfor<0, G ? 3 : 9>([&](auto I) { constexpr int ii = (G ? 9 : 0) + I; const int fb = G ? 3 * m + I : I; u32x4 o;
          sfor<0, 4>([&](auto E) { constexpr int e = E;
            o[e] = pk2(__builtin_fmaf(blo(zr[ii][e]), sc[2 * e], of[2 * e]), __builtin_fmaf(bhi(zr[ii][e]), sc[2 * e + 1], of[2 * e + 1])); });
          st16(rw, WS_Z2 + (j * 64 + 4 * i + g) * 9216 + fb * 1024 + l * 16, o);
          if constexpr (I % 3 == 2) tick(); }); });
      STAMP(3);
      tick(); tick();
      VMW(0);
      if (l == 0) st_flag(ws, WS_FZ2 + (j * 16 + i) * 32 + m * 8, tag);
      STAMP(4);
    }
    upto(NB_C0);
    const f32x16 z16 = {0.f, 0.f, 0.f, 0.f, 0.f, 0.f, 0.f, 0.f, 0.f, 0.f, 0.f, 0.f, 0.f, 0.f, 0.f, 0.f};
    const unsigned opo = t32 * 80 + h * 16, opo2 = t32 * 80 + 64;
    if (m == 0) {
      lim = NB_G2 + 29;
      bf16x8 sel[2];
      sfor<0, 2>([&](auto O) { u32x4 v; sfor<0, 4>([&](auto E) { const int k0 = 16 * O + 8 * h + 2 * E; v[(int)E] = t32 == k0 ? 0x3f80u : (t32 == k0 + 1 ? 0x3f800000u : 0u); });
        sel[O] = __builtin_bit_cast(bf16x8, v); });
      f32x16 kv[3][2];
      sfor<0, 6>([&](auto I) { kv[I / 2][I % 2] = z16; });
      const unsigned vao = LDS_V + (4 * h + ((l >> 2) & 3)) * 80 + ((l >> 4) & 1) * 32 + (l & 3) * 8, vbo = LDS_V + (4 * h + ((l >> 2) & 3)) * 80 + 64 + (l & 1) * 8;
      for (int g = 0; g < 4; ++g) {
        const LAS char* kp = smem + LDS_K + g * 2560;
        const bf16x8 k0 = lds_b128(kp + opo), k1 = lds_b128(kp + opo + 32), k2 = lds_b128(kp + opo2);
        f32x16 p0 = MFMA32(wf[0][0], k0, z16), p1 = MFMA32(wf[1][0], k0, z16);
        p0 = MFMA32(wf[0][1], k1, p0); p1 = MFMA32(wf[1][1], k1, p1);
        p0 = MFMA32(wf[0][2], k2, p0); p1 = MFMA32(wf[1][2], k2, p1);
        u32x4 ph[5];
        hedgehog(p0, p1, ph, tick);
        bf16x8 va[2], vb[2];
        sfor<0, 2>([&](auto S) { const LAS char* vp = smem + g * 2560 + S * 1280; va[S] = lds_tr(vp + vao, vp + vao + 640); vb[S] = lds_tr(vp + vbo, vp + vbo + 640); });
        sfor<0, 3>([&](auto A) { constexpr int a = A;
          f32x16 T = MFMA32(__builtin_bit_cast(bf16x8, ph[2 * a]), sel[0], z16);
          if constexpr (a < 2) T = MFMA32(__builtin_bit_cast(bf16x8, ph[2 * a + 1]), sel[1], T);
          sfor<0, 2>([&](auto S) { constexpr int s = S; u32x4 tp;
            sfor<0, 4>([&](auto E) { tp[(int)E] = pk2(T[8 * s + 2 * E], T[8 * s + 2 * E + 1]); });
            kv[a][0] = MFMA32(__builtin_bit_cast(bf16x8, tp), va[s], kv[a][0]);
            kv[a][1] = MFMA32(__builtin_bit_cast(bf16x8, tp), vb[s], kv[a][1]); });
          if constexpr (a != 1) tick(); });
      }
      STAMP(5);
      const unsigned kb = WS_KVP + (j * 16 + i) * 6144;
      sfor<0, 10>([&](auto I) { constexpr int a = I / 4, g = I % 4, hd = g & 1, s = 2 * a + (g >> 1);
        sfor<0, 2>([&](auto Bk) { constexpr int bk = Bk;
          int d; bool ok;
          if constexpr (bk == 0) { d = t32 - 18 * hd; ok = d >= 0 && d < 18; }
          else { d = t32 == 4 ? 18 : 14 + t32; ok = t32 == 4 || (hd == 1 && t32 < 4); }
          if constexpr (a == 2) ok = ok && h == 0;
          const f32x4 v = {kv[a][bk][4 * g], kv[a][bk][4 * g + 1], kv[a][bk][4 * g + 2], kv[a][bk][4 * g + 3]};
          if (ok) st16(rw, kb + ((hd * 19 + d) * 40 + 8 * s + 4 * h) * 4, __builtin_bit_cast(u32x4, v)); });
        if constexpr (I % 4 == 3) tick(); });
      tick();
      VMW(0);
      if (l == 0) st_flag(ws, WS_FKVP + (j * 16 + i) * 8, tag);
      tick();
      wait_tick(WS_FKVP + (j * 16 + c) * 8);
      STAMP(6);
      { const int x = 24 * i + l; const bool act = l < 24 && x < 380; const bool pad = (x % 10) == 9;
        f32x4 s = {0.f, 0.f, 0.f, 0.f};
        if (act && !pad) { u32x4 t[16];
          sfor<0, 16>([&](auto I) { t[I] = ld16s(rw, WS_KVP + x * 16, (j * 16 + I) * 6144); });
          sfor<0, 16>([&](auto I) { s += __builtin_bit_cast(f32x4, t[I]); }); }
        if (act) { const u32x2 o = {pk2(s[0], s[1]), pk2(s[2], s[3])}; st8(rw, WS_KVB + j * 3072 + x * 8, o); } }
      tick();
      VMW(0);
      if (l == 0) st_flag(ws, WS_FKVB + (j * 16 + i) * 8, tag);
      STAMP(7);
      upto(NB_G2DONE + 9);
    } else {
      bf16x8 qf[2][3];
      sfor<0, 2>([&](auto U) { const LAS char* qp = smem + LDS_Q + (2 * (m - 1) + U) * 2560;
        qf[U][0] = lds_b128(qp + opo); qf[U][1] = lds_b128(qp + opo + 32); qf[U][2] = lds_b128(qp + opo2); });
      LGKM0();
      lim = NB_G2 + 29;
      u32x4 ph[2][5];
      sfor<0, 2>([&](auto U) {
        f32x16 p0 = MFMA32(wf[0][0], qf[U][0], z16), p1 = MFMA32(wf[1][0], qf[U][0], z16);
        p0 = MFMA32(wf[0][1], qf[U][1], p0); p1 = MFMA32(wf[1][1], qf[U][1], p1);
        p0 = MFMA32(wf[0][2], qf[U][2], p0); p1 = MFMA32(wf[1][2], qf[U][2], p1);
        hedgehog(p0, p1, ph[U], tick); });
      STAMP(5);
      wait_tick(WS_FKVB + (j * 16 + c) * 8);
      STAMP(6);
      u32x4 kf[2][5];
      { const int hp = (t32 >> 2) & 1, reg = (t32 & 3) + 4 * (t32 >> 3);
        sfor<0, 10>([&](auto I) { constexpr int bk = I / 5, s = I % 5; const bool ok = hp == h && (bk == 0 || reg < 3);
          kf[bk][s] = ld16(rw, ok ? WS_KVB + j * 3072 + ((h * 19 + 16 * bk + reg) * 40 + 8 * s) * 2 : 0x7ffffff0u); }); }
      tick();
      sfor<0, 2>([&](auto U) { const int g = 2 * (m - 1) + U;
        f32x16 n0 = z16, n1 = z16;
        sfor<0, 5>([&](auto S) { n0 = MFMA32(__builtin_bit_cast(bf16x8, kf[0][S]), __builtin_bit_cast(bf16x8, ph[U][S]), n0);
          n1 = MFMA32(__builtin_bit_cast(bf16x8, kf[1][S]), __builtin_bit_cast(bf16x8, ph[U][S]), n1); });
        tick();
        const float r = 1.0f / (n1[2] + 1e-6f);
        u32x4 o0, o1;
        sfor<0, 4>([&](auto E) { o0[(int)E] = pk2(n0[2 * E] * r, n0[2 * E + 1] * r); o1[(int)E] = pk2(n0[8 + 2 * E] * r, n0[9 + 2 * E] * r); });
        const unsigned ob = WS_OG + ((8 * i + 2 * g + (t32 >> 4)) * 16 + j) * 1024 + (32 * h + c) * 16;
        st16(rw, ob, o0); st16(rw, ob + 256, o1);
        st4(rw, WS_OL + j * 16384 + (128 * i + 32 * g + t32) * 8 + h * 4, pk2(n1[0] * r, n1[1] * r));
        tick(); });
      VMW(0);
      if (l == 0) st_flag(ws, WS_FO + (i * 16 + j) * 16 + (m - 1) * 8, tag);
      STAMP(7);
      upto(NB_G2DONE + 9);
    }
  } else {
    const u64 tag = *(const u64*)(ws + WS_EPOCH) + 1;
    bf16x8 xf[2][18];
    { unsigned aoff[2]; aoff[0] = (w * 32 + c) * 128 + ((q ^ ((c >> 1) & 7)) << 4); aoff[1] = aoff[0] ^ 64;
    float s1[2] = {0.f, 0.f}, s2[2] = {0.f, 0.f};
    const bf16x2 ones = {(__bf16)1.0f, (__bf16)1.0f};
    LGKM0();
    sfor<0, 9>([&](auto N) { constexpr int n = N;
      BAR();
      sfor<0, 4>([&](auto I) { constexpr int b = I & 1, ks = I >> 1; xf[b][2 * n + ks] = lds_b128(smem + (n % 6) * SL1 + aoff[ks] + b * 2048); });
      sfor<0, 4>([&](auto I) { constexpr int b = I & 1, ks = I >> 1; const u32x4 v = __builtin_bit_cast(u32x4, xf[b][2 * n + ks]);
        sfor<0, 4>([&](auto E) { const unsigned ve = v[(int)E]; const bf16x2 p = __builtin_bit_cast(bf16x2, ve);
          s1[b] = __builtin_amdgcn_fdot2_f32_bf16(p, ones, s1[b], false); s2[b] = __builtin_amdgcn_fdot2_f32_bf16(p, p, s2[b], false); }); });
      SCHED(); });
    BAR();
    STAMP(1);
    sfor<0, 2>([&](auto B) { constexpr int b = B;
      const float mu = quad_rows_sum(s1[b]) * (1.f / 576.f), e2 = quad_rows_sum(s2[b]) * (1.f / 576.f);
      const float rs = __builtin_amdgcn_rsqf(fmaxf(e2 - mu * mu, 0.f) + 1e-5f), of = -mu * rs;
      sfor<0, 18>([&](auto S) { u32x4 v = __builtin_bit_cast(u32x4, xf[b][S]);
        sfor<0, 4>([&](auto E) { f32x2 t = {blo(v[(int)E]), bhi(v[(int)E])}; t = t * rs + of; v[(int)E] = pk2(t.x, t.y); });
        xf[b][S] = __builtin_bit_cast(bf16x8, v); SCHED(); }); });
    STAMP(2); }
    f32x4 acc[9][2];
    gemm_pass<0, 9, 3>(xf, acc, smem);
    { const int l = lane_id(), c = l & 15, q = l >> 4;
      STAMP(3);
      f32x2 t1[4], t2[4];
      sfor<0, 9>([&](auto F) { constexpr int f = F; u32x4 o;
        sfor<0, 4>([&](auto E) { constexpr int e = E; const f32x2 x = {acc[f][e >> 1][2 * (e & 1)], acc[f][e >> 1][2 * (e & 1) + 1]};
          const f32x2 y = gelu2(x);
          if constexpr (f == 0) { t1[e] = y; t2[e] = y * y; } else { t1[e] += y; t2[e] += y * y; }
          o[e] = pk2(y.x, y.y); });
        *(LAS u32x4*)(smw + LDS_Z + (w * 9 + f) * 1024 + l * 16) = o; SCHED(); });
      float a1 = 0.f, a2 = 0.f;
      sfor<0, 8>([&](auto E) { constexpr int e = E; const float u1 = row_sum16(t1[e >> 1][e & 1]), u2 = row_sum16(t2[e >> 1][e & 1]);
        if ((c & 7) == e) { a1 = u1; a2 = u2; } });
      const u32x4 gr = {__float_as_uint(a1), (unsigned)tag, __float_as_uint(a2), (unsigned)(tag >> 32)};
      if (c < 8) st16(rw, WS_STAT + ((i * 16 + j) * 128 + w * 32 + q * 8 + c) * 16, gr);
      LGKM0();
      STAMP(4);
    }
    gemm_pass<1, 7, 0>(xf, acc, smem);
    { const int l = lane_id(), c = l & 15, q = l >> 4;
    STAMP(5);
    sfor<0, 2>([&](auto B) { constexpr int b = B; const unsigned ro = (w * 32 + b * 16 + c) * 80;
      sfor<0, 6>([&](auto F) { constexpr int f = F; constexpr int base = (f >> 1) == 0 ? LDS_Q : ((f >> 1) == 1 ? LDS_K : LDS_V);
        const u32x2 o = {pk2(acc[f][b][0], acc[f][b][1]), pk2(acc[f][b][2], acc[f][b][3])};
        *(LAS u32x2*)(smw + base + ro + (f & 1) * 32 + q * 8) = o; });
      u32x2 o = {pk2(acc[6][b][0], acc[6][b][1]), pk2(acc[6][b][2], acc[6][b][3])};
      if (q == 3) { o.x = 0x3f80u; o.y = 0u; }
      *(LAS u32x2*)(smw + ro + (q == 0 ? LDS_Q + 64 : (q == 1 ? LDS_K + 64 : (q == 2 ? LDS_V + 64 : LDS_V + 72)))) = o; });
    LGKM0();
    STAMP(6); }
    gemm_pass<2, 9, 3>(xf, acc, smem);
    const int l = lane_id(), c = l & 15, q = l >> 4;
    STAMP(7);
    u32x2 z1[9][2];
    float bias[2];
    sfor<0, 2>([&](auto B) { bias[B] = blo(P.bsp[i * 128 + w * 32 + 8 * (c >> 2) + 4 * B + (c & 3)]); });
    sfor<0, 18>([&](auto I) { constexpr int f = I >> 1, b = I & 1;
      const f32x2 y0 = gelu2((f32x2){acc[f][b][0], acc[f][b][1]}), y1 = gelu2((f32x2){acc[f][b][2], acc[f][b][3]});
      z1[f][b] = (u32x2){pk2(y0.x, y0.y), pk2(y1.x, y1.y)}; });
    sfor<0, 18>([&](auto I) { constexpr int f = I >> 1, b = I & 1; acc[f][b] = (f32x4){bias[b], bias[b], bias[b], bias[b]}; });
    STAMP(8);
    {
      unsigned ao[4][2], bo[4];
      sfor<0, 4>([&](auto U) { ao[U][0] = U * SL2 + (w * 32 + c) * 128 + ((q ^ ((c >> 1) & 7)) << 4); ao[U][1] = ao[U][0] ^ 64; bo[U] = U * SL2 + 16384 + l * 16;
        pin(ao[U][0]); pin(ao[U][1]); pin(bo[U]); });
      bf16x8 fa0[2], fb0[9], fa1[2], fb1[9];
      auto rd = [&](bf16x8(&fa)[2], bf16x8(&fb)[9], auto U, auto KS) { constexpr int u = U, ks = KS;
        fa[0] = lds_b128(smem + ao[u][ks]); fa[1] = lds_b128(smem + ao[u][ks] + 2048);
        sfor<0, 9>([&](auto F) { fb[F] = lds_b128(smem + bo[u] + (ks * 9 + F) * 1024); }); };
      const std::integral_constant<int, 0> I0; const std::integral_constant<int, 1> I1;
      auto mm = [&](bf16x8(&fa)[2], bf16x8(&fb)[9]) {
        sfor<0, 9>([&](auto F) { constexpr int f = F, b0 = f & 1, b1 = b0 ^ 1;
          acc[f][b0] = MFMA16(fb[f], fa[b0], acc[f][b0]); acc[f][b1] = MFMA16(fb[f], fa[b1], acc[f][b1]); });
        sfor<0, 11>([&](auto) { __builtin_amdgcn_sched_group_barrier(0x8, 1, 0); __builtin_amdgcn_sched_group_barrier(0x100, 1, 0); });
        __builtin_amdgcn_sched_group_barrier(0x8, 7, 0); SCHED(); };
      SCHED(); BAR(); SCHED();
      STAMP(9);
      rd(fa0, fb0, I0, I0);
      for (int tt = 0; tt < 8; ++tt) {
        sfor<0, 4>([&](auto U) { constexpr int u = U;
          rd(fa1, fb1, U, I1); mm(fa0, fb0);
          LGKM0(); BAR(); SCHED();
          rd(fa0, fb0, std::integral_constant<int, (u + 1) & 3>{}, I0); mm(fa1, fb1); });
      }
    }
    STAMP(10);
    {
      u32x2 up[9][2];
      sfor<0, 18>([&](auto I) { constexpr int f = I >> 1, b = I & 1;
        up[f][b] = (u32x2){pk2(acc[f][b][0] * blo(z1[f][b].x), acc[f][b][1] * bhi(z1[f][b].x)), pk2(acc[f][b][2] * blo(z1[f][b].y), acc[f][b][3] * bhi(z1[f][b].y))}; });
      const unsigned ub = WS_U + ((i * 4 + w) * 16 + j) * 9216 + l * 16;
      sfor<0, 8>([&](auto I) { constexpr int b = I >> 2, a = I & 3;
        st16(rw, ub + I * 1024, (u32x4){up[2 * a][b].x, up[2 * a][b].y, up[2 * a + 1][b].x, up[2 * a + 1][b].y}); });
      st16(rw, ub + 8192, (u32x4){up[8][0].x, up[8][0].y, up[8][1].x, up[8][1].y});
      VMW(0);
      if (l == 0) st_flag(ws, WS_FU + ((i * 16 + j) * 4 + w) * 8, tag);
    }
    STAMP(11);
    const int th = j & 1, cg = j >> 1;
    f32x4 o3[4][4], oh[2][4];
    sfor<0, 16>([&](auto I) { o3[I >> 2][I & 3] = (f32x4){0.f, 0.f, 0.f, 0.f}; });
    sfor<0, 8>([&](auto I) { oh[I >> 2][I & 3] = (f32x4){0.f, 0.f, 0.f, 0.f}; });
    auto step = [&](auto ST, const bf16x8(&wv)[4], const bf16x8& wh, const u32x4(&uf)[4]) {
      sfor<0, 4>([&](auto T) { const bf16x8 u = __builtin_bit_cast(bf16x8, uf[T]);
        sfor<0, 4>([&](auto Bk) { o3[Bk][T] = MFMA16(wv[Bk], u, o3[Bk][T]); });
        oh[ST][T] = MFMA16(wh, u, oh[ST][T]); }); };
    for (int pr = w; pr < 9; pr += 4) {
      const unsigned wb = WS_W3 + (cg * 45 + 36 + pr) * 9216 + l * 16;
      u32x4 wr[9], uf[2][4];
      sfor<0, 9>([&](auto I) { wr[I] = ld16(rw, wb + I * 1024); });
      if (pr < 8) sfor<0, 8>([&](auto I) { uf[I >> 2][I & 3] = ld16(rw, WS_OG + ((8 * i + 4 * th + (I & 3)) * 16 + 2 * pr + (I >> 2)) * 1024 + l * 16); });
      else sfor<0, 8>([&](auto I) { const unsigned a = WS_OL + (8 * (I >> 2) + 2 * q) * 16384 + (128 * i + 64 * th + 16 * (I & 3) + c) * 8;
        const u32x2 x0 = ld8(rw, a), x1 = ld8(rw, a + 16384); uf[I >> 2][I & 3] = (u32x4){x0.x, x0.y, x1.x, x1.y}; });
      sfor<0, 2>([&](auto S) { bf16x8 wv[4]; sfor<0, 4>([&](auto Bk) { wv[Bk] = __builtin_bit_cast(bf16x8, wr[4 * S + Bk]); });
        step(S, wv, __builtin_bit_cast(bf16x8, wr[8]), uf[S]); });
    }
    STAMP(12);
    wait_flags(ws, WS_FU + ((i * 16 + 4 * w + ((l >> 1) & 3)) * 4 + 2 * th + (l & 1)) * 8, tag);
    STAMP(13);
    {
      constexpr int UD = 3;
      u32x4 uf[UD + 1][4];
      const unsigned ub = WS_U + ((i * 4 + 2 * th) * 16 + 4 * w) * 9216 + l * 16;
      auto ldu = [&](auto S) { constexpr int s = S, jp = s / 9, s9 = s % 9;
        sfor<0, 4>([&](auto T) { constexpr int t = T;
          constexpr unsigned o = s9 == 4 ? ((t >> 1) * 16 + 2 * jp + (t & 1)) * 9216 + 8192
                                         : ((t >> 1) * 16 + 2 * jp + (s9 > 4)) * 9216 + ((t & 1) * 4 + (s9 > 4 ? s9 - 5 : s9)) * 1024;
          uf[s % (UD + 1)][t] = ld16s(rw, ub, o); }); };
      sfor<0, UD>(ldu);
      bf16x8 wh;
      sfor<0, 18>([&](auto S) { constexpr int s = S, r = s >> 1, st = s & 1;
        if constexpr (s + UD < 18) ldu(std::integral_constant<int, s + UD>{});
        const LAS char* sp = smem + (r & 3) * SL3 + w * 9216 + l * 16;
        if constexpr (st == 0) { SCHED(); LGKM0(); BAR(); SCHED(); wh = lds_b128(sp + 8192); }
        bf16x8 wv[4];
        sfor<0, 4>([&](auto Bk) { wv[Bk] = lds_b128(sp + (st * 4 + Bk) * 1024); });
        if constexpr (s % 9 == 4) { u32x4(&x)[4] = uf[s % (UD + 1)];
          const u32x4 y[4] = {{x[0].x, x[0].y, x[1].x, x[1].y}, {x[0].z, x[0].w, x[1].z, x[1].w}, {x[2].x, x[2].y, x[3].x, x[3].y}, {x[2].z, x[2].w, x[3].z, x[3].w}};
          step(std::integral_constant<int, st>{}, wv, wh, y);
        } else step(std::integral_constant<int, st>{}, wv, wh, uf[s % (UD + 1)]);
        SCHED(); });
    }
    STAMP(14);
    sfor<0, 4>([&](auto T) { LAS char* rp = smw + 2 * SL3 + w * 21504 + (T * 16 + c) * 336 + q * 16;
      sfor<0, 4>([&](auto Bk) { *(LAS f32x4*)(rp + Bk * 64) = o3[Bk][T]; });
      *(LAS f32x4*)(rp + 256) = q < 2 ? oh[0][T] : oh[1][T]; });
    LGKM0();
  }
  BAR();
  {
    const int th = j & 1, cg = j >> 1;
    const int l = lane_id();
    for (int it = w * 64 + l; it < 576; it += 512) {
      const int row = it / 9, ch = it - row * 9, cc = row & 15, tb = row >> 4;
      const int tok = i * 128 + th * 64 + (tb >> 1) * 32 + 8 * (cc >> 2) + 4 * (tb & 1) + (cc & 3);
      const size_t go = (size_t)tok * 576 + cg * 72 + ch * 8;
      const u32x4 xr = *(const u32x4*)(P.x + go);
      const LAS char* rp = smem + 2 * SL3 + row * 336 + ch * 32;
      f32x4 a = {0.f, 0.f, 0.f, 0.f}, b = {0.f, 0.f, 0.f, 0.f};
      sfor<0, 4>([&](auto W) { a += *(const LAS f32x4*)(rp + W * 21504); b += *(const LAS f32x4*)(rp + W * 21504 + 16); });
      if (ch == 8) sfor<0, 4>([&](auto W) { a += *(const LAS f32x4*)(rp + W * 21504 + 32); b += *(const LAS f32x4*)(rp + W * 21504 + 48); });
      const u32x4 o = {pk2(a[0] + blo(xr.x), a[1] + bhi(xr.x)), pk2(a[2] + blo(xr.y), a[3] + bhi(xr.y)),
                       pk2(b[0] + blo(xr.z), b[1] + bhi(xr.z)), pk2(b[2] + blo(xr.w), b[3] + bhi(xr.w))};
      *(u32x4*)(P.out + go) = o;
    }
    STAMP(15);
  }
}

extern "C" void kernel_launch(void* const* d_in, const int* in_sizes, int n_in, void* d_out, int out_size, void* d_ws, size_t ws_size,
                              hipStream_t stream) {
  Params p;
  p.x = (const u16*)d_in[0]; p.wqkv = (const u16*)d_in[1]; p.wphi = (const u16*)d_in[2]; p.wo = (const u16*)d_in[3];
  p.win = (const u16*)d_in[5]; p.wsp = (const u16*)d_in[6]; p.bsp = (const u16*)d_in[7]; p.wout = (const u16*)d_in[8];
  p.out = (u16*)d_out; p.ws = (char*)d_ws;
  (void)hipFuncSetAttribute((const void*)fused_block, hipFuncAttributeMaxDynamicSharedMemorySize, LDS_TOTAL);
  fused_block<<<256, 512, LDS_TOTAL, stream>>>(p);
}
```

```cpp
#include <hip/hip_runtime.h>
#include <type_traits>
#include <utility>

typedef unsigned short u16;
typedef unsigned long long u64;
typedef __attribute__((ext_vector_type(8))) __bf16 bf16x8;
typedef __attribute__((ext_vector_type(4))) __bf16 bf16x4;
typedef __attribute__((ext_vector_type(2))) __bf16 bf16x2;
typedef __attribute__((ext_vector_type(4))) short s16x4;
typedef __attribute__((ext_vector_type(2))) float f32x2;
typedef __attribute__((ext_vector_type(4))) float f32x4;
typedef __attribute__((ext_vector_type(16))) float f32x16;
typedef __attribute__((ext_vector_type(2))) unsigned u32x2;
typedef __attribute__((ext_vector_type(4))) unsigned u32x4;
#define GAS __attribute__((address_space(1)))
#define LAS __attribute__((address_space(3)))
#define DI __device__ __forceinline__
#define SCHED() __builtin_amdgcn_sched_barrier(0)
#define BAR() do { asm volatile("" ::: "memory"); __builtin_amdgcn_s_barrier(); asm volatile("" ::: "memory"); } while (0)
#define LGKM0() __builtin_amdgcn_s_waitcnt(0xC07F)
#define VMW(n) asm volatile("s_waitcnt vmcnt(%0)" ::"i"((n) > 63 ? 63 : (n)) : "memory")
#define MFMA16(a, b, c) __builtin_amdgcn_mfma_f32_16x16x32_bf16((a), (b), (c), 0, 0, 0)
#define MFMA32(a, b, c) __builtin_amdgcn_mfma_f32_32x32x16_bf16((a), (b), (c), 0, 0, 0)

template <int B, class F, int... I> DI void sfor_impl(F& f, std::integer_sequence<int, I...>) { (f(std::integral_constant<int, B + I>{}), ...); }
template <int B, int E, class F> DI void sfor(F&& f) { sfor_impl<B>(f, std::make_integer_sequence<int, (E > B ? E - B : 0)>{}); }

constexpr unsigned WS_EPOCH = 0;
constexpr unsigned WS_FZ2 = 4096;
constexpr unsigned WS_FKVP = WS_FZ2 + 8192;
constexpr unsigned WS_FKVB = WS_FKVP + 4096;
constexpr unsigned WS_FO = WS_FKVB + 2048;
constexpr unsigned WS_FU = WS_FO + 8192;
constexpr unsigned WS_STAT = 65536;
constexpr unsigned WS_KVP = WS_STAT + 524288;
constexpr unsigned WS_KVB = WS_KVP + 256 * 8192;
constexpr unsigned WS_OL = WS_KVB + 16 * 4096;
constexpr unsigned WS_OG = WS_OL + 16 * 16384;
constexpr unsigned WS_W3 = WS_OG + 128 * 16384;
constexpr unsigned WS_Z2 = WS_W3 + 8 * 45 * 9216;
constexpr unsigned WS_U = WS_Z2 + 16 * 64 * 9216;
constexpr unsigned WS_DBG = WS_U + 16 * 4 * 16 * 9216;
constexpr unsigned WS_END = WS_DBG + 256 * 8 * 32 * 8;

constexpr int SL1 = 18432;
constexpr int SL2 = 34816;
constexpr int SL3 = 36864;
constexpr int LDS_AR = 110592, AR = 13312;
constexpr int AR_Q = 0, AR_V = 2560, AR_K = 5120, AR_P = 7680;
constexpr int LDS_WPHI = 49152;
constexpr int LDS_SCR = LDS_AR + AR - 256;
constexpr int LDS_KVB = 159744;
constexpr int LDS_W8 = 147456;
constexpr int LDS_TOTAL = 163840;

constexpr int NB_XDONE = 1, NB_A0 = 2, NB_ADONE = 11, NB_BDONE = 21, NB_C0 = 23, NB_CDONE = 32, NB_G2 = 33  , NB_KV = NB_G2 + 13, NB_G2DONE = 65, NB_S0 = 67, NB_LAST = 73;

#define DEV_CLK "s_memrealtime"
#define ST_DECL do {} while (0)
#define STAMP(k) do {} while (0)
#define ST_FLUSH() do {} while (0)
#define TR_DECL do {} while (0)
#define TR_ARR() do {} while (0)
#define TBAR() BAR()
#define TR_FLUSH() do {} while (0)
template <int OFF, int SC1> DI void glds_m0(const char* sbase, unsigned voff, unsigned ldsaddr) {
  if constexpr (SC1) asm volatile("s_mov_b32 m0, %2\n\ts_nop 0\n\tglobal_load_lds_dwordx4 %0, %1 offset:%3 sc1" ::"v"(voff), "s"(sbase), "s"(ldsaddr), "i"(OFF) : "memory");
  else asm volatile("s_mov_b32 m0, %2\n\ts_nop 0\n\tglobal_load_lds_dwordx4 %0, %1 offset:%3" ::"v"(voff), "s"(sbase), "s"(ldsaddr), "i"(OFF) : "memory");
}
template <int OFF, int SC1> DI void glds(const char* sbase, unsigned voff) {
  if constexpr (SC1) asm volatile("global_load_lds_dwordx4 %0, %1 offset:%2 sc1" ::"v"(voff), "s"(sbase), "i"(OFF) : "memory");
  else asm volatile("global_load_lds_dwordx4 %0, %1 offset:%2" ::"v"(voff), "s"(sbase), "i"(OFF) : "memory");
}
DI void touch(const char* sbase, unsigned voff, unsigned ldsaddr) {
  asm volatile("s_mov_b32 m0, %2\n\ts_nop 0\n\tglobal_load_lds_dword %0, %1" ::"v"(voff), "s"(sbase), "s"(ldsaddr) : "memory");
}
template <int NP> DI void issue_tile(const char* sbase, const unsigned (&v)[NP], unsigned lds) {
  sfor<0, NP>([&](auto P) {
    constexpr int p = P;
    if constexpr ((p & 3) == 0) glds_m0<0, 0>(sbase, v[p], lds + p * 1024);
    else glds<(p & 3) * 1024, 0>(sbase, v[p]);
  });
}
template <int NP> DI void issue_lin(const char* sbase, unsigned vl, unsigned lds) {
  sfor<0, NP>([&](auto P) {
    constexpr int p = P;
    if constexpr ((p & 3) == 0) glds_m0<0, 1>(sbase + p * 1024, vl, lds + p * 1024);
    else glds<(p & 3) * 1024, 1>(sbase + (p & ~3) * 1024, vl);
  });
}
DI bf16x8 lds_b128(const LAS char* p) { return *(const LAS bf16x8*)p; }
DI bf16x8 lds_tr(const LAS char* p0, const LAS char* p1) {
  bf16x4 a = __builtin_amdgcn_ds_read_tr16_b64_v4bf16((LAS bf16x4*)p0);
  bf16x4 b = __builtin_amdgcn_ds_read_tr16_b64_v4bf16((LAS bf16x4*)p1);
  return __builtin_shufflevector(a, b, 0, 1, 2, 3, 4, 5, 6, 7);
}
DI unsigned pk2(float a, float b) { f32x2 v = {a, b}; return __builtin_bit_cast(unsigned, __builtin_convertvector(v, bf16x2)); }
DI unsigned pk2s(float a, float b) { unsigned r; asm("v_cvt_pk_bf16_f32 %0, %1, %2" : "=v"(r) : "v"(a), "v"(b)); return r; }
DI float blo(unsigned u) { return __uint_as_float(u << 16); }
DI float bhi(unsigned u) { return __uint_as_float(u & 0xffff0000u); }
template <int CTRL> DI float dpp_add(float x) { return x + __int_as_float(__builtin_amdgcn_update_dpp(0, __float_as_int(x), CTRL, 0xf, 0xf, true)); }
template <int ST> DI void dpp_fold(float& a, float b) {
  if constexpr (ST == 0) asm volatile("v_add_f32_dpp %0, %0, %0 row_ror:8 row_mask:0xf bank_mask:0x3\n\tv_add_f32_dpp %0, %1, %1 row_ror:8 row_mask:0xf bank_mask:0xc" : "+v"(a) : "v"(b));
  else asm volatile("v_add_f32_dpp %0, %0, %0 row_half_mirror row_mask:0xf bank_mask:0x5\n\tv_add_f32_dpp %0, %1, %1 row_half_mirror row_mask:0xf bank_mask:0xa" : "+v"(a) : "v"(b));
}
DI float row_sum16(float x) { x = dpp_add<0x128>(x); x = dpp_add<0x124>(x); x = dpp_add<0x122>(x); return dpp_add<0x121>(x); }
DI float quad_rows_sum(float x) {
  auto a = __builtin_amdgcn_permlane32_swap(__float_as_uint(x), __float_as_uint(x), false, false);
  x = __uint_as_float(a[0]) + __uint_as_float(a[1]);
  auto b = __builtin_amdgcn_permlane16_swap(__float_as_uint(x), __float_as_uint(x), false, false);
  return __uint_as_float(b[0]) + __uint_as_float(b[1]);
}
DI f32x2 gelu2(f32x2 x) {
  const float k0 = -2.302208198f, k1 = -0.1029432397f;
  f32x2 t = x * ((x * x) * k1 + k0);
  f32x2 d;
  d.x = __builtin_amdgcn_exp2f(t.x); d.y = __builtin_amdgcn_exp2f(t.y);
  d = d + 1.0f;
  f32x2 r;
  r.x = __builtin_amdgcn_rcpf(d.x); r.y = __builtin_amdgcn_rcpf(d.y);
  return x * r;
}
DI int lane_id() { int l; asm volatile("v_mbcnt_lo_u32_b32 %0, -1, 0\n\tv_mbcnt_hi_u32_b32 %0, -1, %0" : "=v"(l)); return l & 63; }
template <class T> DI void pin(T& x) { asm volatile("" : "+v"(x)); }
typedef __amdgpu_buffer_rsrc_t rsrc_t;
DI void st16(rsrc_t r, unsigned off, u32x4 v) { __builtin_amdgcn_raw_buffer_store_b128(v, r, off, 0, 16); }
DI void st8(rsrc_t r, unsigned off, u32x2 v) { __builtin_amdgcn_raw_buffer_store_b64(v, r, off, 0, 16); }
DI void st4(rsrc_t r, unsigned off, unsigned v) { __builtin_amdgcn_raw_buffer_store_b32(v, r, off, 0, 16); }
DI u32x4 ld16(rsrc_t r, unsigned off) { return __builtin_amdgcn_raw_buffer_load_b128(r, off, 0, 16); }
DI u32x4 ld16s(rsrc_t r, unsigned off, unsigned soff) { return __builtin_amdgcn_raw_buffer_load_b128(r, off, soff, 16); }
DI u32x2 ld8(rsrc_t r, unsigned off) { return __builtin_amdgcn_raw_buffer_load_b64(r, off, 0, 16); }
DI u64 ld_flag(char* ws, unsigned off) { return __hip_atomic_load((GAS u64*)(ws + off), __ATOMIC_RELAXED, __HIP_MEMORY_SCOPE_AGENT); }
DI void st_flag(char* ws, unsigned off, u64 tag) { __hip_atomic_store((GAS u64*)(ws + off), tag, __ATOMIC_RELAXED, __HIP_MEMORY_SCOPE_AGENT); }
template <bool FORCE> DI void flag_when_drained(int& pend, const char* fp, u64 tag) {
  unsigned st, z; u64 d;
  asm volatile("s_cmp_eq_u32 %0, 0\n\ts_cbranch_scc1 1f\n\ts_getreg_b32 %1, hwreg(HW_REG_IB_STS)\n\ts_and_b32 %1, %1, %6\n\ts_cbranch_scc1 1f\n\t"
               "v_mov_b32 %2, 0\n\tv_mov_b64 %3, %5\n\ts_waitcnt vmcnt(0)\n\ts_mov_b64 exec, 1\n\tglobal_store_dwordx2 %2, %3, %4 sc1\n\ts_mov_b64 exec, -1\n\ts_mov_b32 %0, 0\n1:"
               : "+s"(pend), "=&s"(st), "=&v"(z), "=&v"(d) : "s"(fp), "s"(tag), "i"(FORCE ? 0u : 0xc000000fu) : "scc", "memory");
}
#define U_POLL_DELAY 64
constexpr int CODE_KB = 64;
constexpr int SPIN_CAP = 1 << 15;
DI void wait_flags(char* ws, unsigned off, u64 tag) {
#pragma nounroll
  for (int it = 0; it < SPIN_CAP; ++it) {
    if (__all(ld_flag(ws, off) == tag)) break;
    __builtin_amdgcn_s_sleep(2);
  }
  asm volatile("" ::: "memory");
}

struct Params {
  const u16 *x, *wqkv, *wphi, *wo, *win, *wsp, *bsp, *wout;
  u16* out;
  char* ws;
};

template <bool FIRST> DI void mfma_t(f32x4& d, const bf16x8& a, const bf16x8& b) {
  if constexpr (FIRST) asm volatile("v_mfma_f32_16x16x32_bf16 %0, %1, %2, 0" : "=&v"(d) : "v"(a), "v"(b));
  else asm volatile("v_mfma_f32_16x16x32_bf16 %0, %1, %2, %0" : "+v"(d) : "v"(a), "v"(b));
}
template <bool FIRST, int OFF> DI void ld16_asm(u32x4& d, const u32x4& r, unsigned v, unsigned so) {
  if constexpr (FIRST) asm volatile("buffer_load_dwordx4 %0, %1, %2, %3 offen offset:%4 sc1 ; HL" : "=&v"(d) : "v"(v), "s"(r), "s"(so), "i"(OFF) : "memory");
  else asm volatile("buffer_load_dwordx4 %0, %1, %2, %3 offen offset:%4 sc1 ; HL" : "+v"(d) : "v"(v), "s"(r), "s"(so), "i"(OFF) : "memory");
}
DI void ld8_asm(u32x2& d, const u32x4& r, unsigned v, unsigned so) { asm volatile("buffer_load_dwordx2 %0, %1, %2, %3 offen sc1 ; HL" : "=&v"(d) : "v"(v), "s"(r), "s"(so) : "memory"); }
DI bf16x4 tr4(const LAS char* p) { return __builtin_amdgcn_ds_read_tr16_b64_v4bf16((LAS bf16x4*)p); }
template <int KIND, int NBLK, int SLOT0, class HK> DI void gemm_pass(const bf16x8 (&xf)[2][18], f32x4 (&acc)[9][2], const LAS char* smem, HK&& hook) {
  constexpr int PD = 4, NF = 18 * NBLK;
  const int l = lane_id();
  unsigned a0[2], a6[2];
  a0[0] = (l >> 2) * 288 + (l & 3) * 8; a0[1] = a0[0] + 3 * SL1; pin(a0[0]); pin(a0[1]);
  if constexpr (KIND == 1) { a6[0] = (l >> 2) * 288 + 72 + 80 * ((l & 3) == 3 ? 2 : (l & 3)); a6[1] = a6[0] + 3 * SL1; pin(a6[0]); pin(a6[1]); }
  bf16x4 fr[PD + 1][2];
  auto rd = [&](auto N, auto H) {
    constexpr int n = N, hf = H, kt = n / (2 * NBLK), ks = (n / NBLK) & 1, f = n % NBLK;
    constexpr int sl = (SLOT0 + kt) % 6, hi = sl / 3, base = (sl % 3) * SL1 + ks * 9216 + hf * 4608;
    if constexpr (n % (2 * NBLK) == 0 && hf == 0) { hook(std::integral_constant<int, kt>{}); BAR(); SCHED(); }
    if constexpr (KIND == 1 && f == 6) fr[n % (PD + 1)][hf] = tr4(smem + a6[hi] + base);
    else fr[n % (PD + 1)][hf] = tr4(smem + a0[hi] + base + (KIND == 1 ? (f >> 1) * 80 + (f & 1) * 32 : f * 32));
    SCHED();
  };
  const std::integral_constant<int, 0> I0; const std::integral_constant<int, 1> I1;
  SCHED();
  sfor<0, PD>([&](auto N) { rd(N, I0); rd(N, I1); });
  sfor<0, NF>([&](auto N) {
    constexpr int n = N, s = n / NBLK, f = n % NBLK, b0 = f & 1, b1 = b0 ^ 1;
    const bf16x8 wv = __builtin_shufflevector(fr[n % (PD + 1)][0], fr[n % (PD + 1)][1], 0, 1, 2, 3, 4, 5, 6, 7);
    if constexpr (KIND == 0) mfma_t<s == 0>(acc[f][b0], xf[b0][s], wv); else mfma_t<s == 0>(acc[f][b0], wv, xf[b0][s]);
    SCHED();
    if constexpr (n + PD < NF) rd(std::integral_constant<int, n + PD>{}, I0);
    if constexpr (KIND == 0) mfma_t<s == 0>(acc[f][b1], xf[b1][s], wv); else mfma_t<s == 0>(acc[f][b1], wv, xf[b1][s]);
    SCHED();
    if constexpr (n + PD < NF) rd(std::integral_constant<int, n + PD>{}, I1);
  });
  BAR();
  asm volatile("s_nop 7" ::: "memory");
  SCHED();
}

template <bool PK, class T> DI void hedgehog(const bf16x8 (&wf)[2][3], const bf16x8 (&op)[3], u32x4 (&ph)[5], T&& tick) {
  const f32x16 z16 = {0.f, 0.f, 0.f, 0.f, 0.f, 0.f, 0.f, 0.f, 0.f, 0.f, 0.f, 0.f, 0.f, 0.f, 0.f, 0.f};
  if constexpr (!PK) { __builtin_amdgcn_s_setprio(1); SCHED(); }
  f32x16 p0 = MFMA32(wf[0][0], op[0], z16), p1 = MFMA32(wf[1][0], op[0], z16);
  p0 = MFMA32(wf[0][1], op[1], p0); p1 = MFMA32(wf[1][1], op[1], p1);
  p0 = MFMA32(wf[0][2], op[2], p0); p1 = MFMA32(wf[1][2], op[2], p1);
  if constexpr (!PK) { SCHED(); __builtin_amdgcn_s_setprio(0); }
  float p[18];
  sfor<0, 16>([&](auto I) { p[I] = p0[(int)I]; });
  p[16] = p1[0]; p[17] = p1[1];
  float mx = p[0], mn = p[0];
  sfor<1, 18>([&](auto I) { mx = fmaxf(mx, p[I]); mn = fminf(mn, p[I]); });
  const float L2E = 1.4426950408889634f;
  const float cx = -mx * L2E, cn = mn * L2E;
  if constexpr (PK) {
    f32x2 a[9], b[9], sa, sb;
    sfor<0, 9>([&](auto I) { const f32x2 x = {p[2 * I], p[2 * I + 1]}; a[I] = x * L2E + cx; b[I] = x * -L2E + cn; }); SCHED();
    sfor<0, 9>([&](auto I) { a[I].x = __builtin_amdgcn_exp2f(a[I].x); a[I].y = __builtin_amdgcn_exp2f(a[I].y); b[I].x = __builtin_amdgcn_exp2f(b[I].x); b[I].y = __builtin_amdgcn_exp2f(b[I].y); }); SCHED();
    sfor<0, 9>([&](auto I) { if constexpr (I == 0) { sa = a[0]; sb = b[0]; } else { sa += a[I]; sb += b[I]; } });
    const float r0 = __builtin_amdgcn_rcpf(sa.x + sa.y), r1 = __builtin_amdgcn_rcpf(sb.x + sb.y); SCHED();
    sfor<0, 9>([&](auto I) { a[I] *= r0; b[I] *= r1; }); SCHED();
    sfor<0, 9>([&](auto I) { constexpr int i = I; ph[i >> 2][i & 3] = pk2(a[i].x, a[i].y); ph[(i + 9) >> 2][(i + 9) & 3] = pk2(b[i].x, b[i].y); });
    ph[4][2] = 0; ph[4][3] = 0;
    return;
  }
  float e[36], s0, s1;
  sfor<0, 18>([&](auto I) { e[I] = __builtin_amdgcn_exp2f(__builtin_fmaf(p[I], L2E, cx)); if constexpr (I == 0) s0 = e[0]; else s0 += e[I]; if constexpr (I == 3) tick(); });
  sfor<0, 18>([&](auto I) { e[18 + I] = __builtin_amdgcn_exp2f(__builtin_fmaf(p[I], -L2E, cn)); if constexpr (I == 0) s1 = e[18]; else s1 += e[18 + I]; if constexpr (I == 3) tick(); });
  const float r0 = __builtin_amdgcn_rcpf(s0), r1 = __builtin_amdgcn_rcpf(s1);
  sfor<0, 18>([&](auto I) { constexpr int i = I; const float r = i < 9 ? r0 : r1; ph[i >> 2][i & 3] = pk2s(e[2 * i] * r, e[2 * i + 1] * r); if constexpr (i == 3) tick(); });
  ph[4][2] = 0; ph[4][3] = 0;
  sfor<0, 5>([&](auto I) { pin(ph[I]); });
}

__global__ __launch_bounds__(512, 2) void fused_block(Params P) {
#if defined(__HIP_DEVICE_COMPILE__)
  extern __shared__ __attribute__((aligned(1024))) char smem_g[];
  u64 pc0; asm volatile("s_getpc_b64 %0" : "=s"(pc0));
  const LAS char* smem = (const LAS char*)smem_g;
  LAS char* smw = (LAS char*)smem_g;
  const unsigned lds0 = (unsigned)(size_t)smem;
  char* ws = P.ws;
  const int bid = blockIdx.x, xcd = bid & 7, rr = bid >> 3;
  const int i = (xcd >> 1) * 4 + (rr >> 3), j = (xcd & 1) * 8 + (rr & 7);
  const int w = __builtin_amdgcn_readfirstlane(threadIdx.x >> 6), l = threadIdx.x & 63;
  const int c = l & 15, q = l >> 4;
  const int rot = (2 * (i & 3) + (i >> 2) + j) % 9;
  const rsrc_t rw = __builtin_amdgcn_make_buffer_rsrc(ws, 0, WS_END, 0x00020000);
  auto get_tag = [&]() { u64 e; asm volatile("s_load_dwordx2 %0, %1, 0x0\n\ts_waitcnt lgkmcnt(0)" : "=&s"(e) : "s"(ws + WS_EPOCH) : "memory"); return e + 1; };
  ST_DECL;
  STAMP(0);

#define MAP3 3
#if MAP3 < 2
  const int i3 = (xcd >> 1) * 4 + (xcd & 1) * 2 + (rr >> 4), th = rr & 1, cg = (rr >> 1) & 7;
  const int off3 = MAP3 == 0 ? (cg + 2 * th + 4 * (rr >> 4)) & 7 : 0;
#else
  const int i3 = i, th = j & 1, cg = j >> 1;
  const int off3 = MAP3 == 2 ? (2 * cg + 2 * i + th) & 7 : 0;
#endif
  auto ktile = [&](int n) { int k = n + rot; return k >= 9 ? k - 9 : k; };
  unsigned vx[4];
  const char* xb = (const char*)P.x + (size_t)(i * 128 + w * 32) * 1152 - 4096;
  auto issue_x = [&](auto N) { constexpr int n = N; issue_tile<4>(xb + ktile(n) * 128, vx, lds0 + (n % 6 < 3 ? 0 : LDS_AR) + (n % 3) * 16384 + w * 4096); };
  sfor<0, 4>([&](auto Pp) { constexpr int p = Pp; const int cc = (p & 1) * 8 + (l >> 3);
    vx[p] = (8 * (cc >> 2) + 4 * (p >> 1) + (cc & 3)) * 1152 + (((l & 7) ^ ((cc >> 1) & 7)) << 4) + 4096 - p * 1024; });
  if (w >= 4) {
    glds_m0<0, 0>((const char*)P.wphi + j * 1296, l * 16, lds0 + LDS_WPHI + (w - 4) * 1536);
    if (l <= 16) glds<1024, 0>((const char*)P.wphi + j * 1296, l * 16);
  }
  asm volatile("" ::: "memory");
  if (w < 4) sfor<0, 4>(issue_x);
  asm volatile("" ::: "memory");

  if (w < 4) {
    const u64 tag = get_tag();
    bf16x8 xf[2][18];
    {
      unsigned aoff[2][2];
      aoff[0][0] = (w * 32 + c) * 128 + ((q ^ ((c >> 1) & 7)) << 4); aoff[0][1] = aoff[0][0] ^ 64;
      aoff[1][0] = aoff[0][0] + LDS_AR; aoff[1][1] = aoff[0][1] + LDS_AR; pin(aoff[1][0]); pin(aoff[1][1]);
      float s1[2] = {0.f, 0.f}, s2[2] = {0.f, 0.f};
      const bf16x2 ones = {(__bf16)1.0f, (__bf16)1.0f};
      sfor<0, 9>([&](auto N) { constexpr int n = N;
        VMW(n < 6 ? 12 : 32 - 4 * n);
        sfor<0, 4>([&](auto I) { constexpr int b = I & 1, ks = I >> 1;
          xf[b][2 * n + ks] = lds_b128(smem + aoff[n % 6 >= 3][ks] + (n % 3) * 16384 + b * 2048); });
        if constexpr (n + 4 < 9) issue_x(std::integral_constant<int, n + 4>{});
        SCHED();
        sfor<0, 16>([&](auto I) { constexpr int b = I & 1, e = (I >> 1) & 3, ks = I >> 3; const u32x4 v = __builtin_bit_cast(u32x4, xf[b][2 * n + ks]);
          const unsigned ve = v[e]; const bf16x2 p = __builtin_bit_cast(bf16x2, ve);
          s1[b] = __builtin_amdgcn_fdot2_f32_bf16(p, ones, s1[b], false); s2[b] = __builtin_amdgcn_fdot2_f32_bf16(p, p, s2[b], false); SCHED(); });
        });
      STAMP(1);
      sfor<0, 2>([&](auto B) { constexpr int b = B;
        const float mu = quad_rows_sum(s1[b]) * (1.f / 576.f), e2 = quad_rows_sum(s2[b]) * (1.f / 576.f);
        const float rs = __builtin_amdgcn_rsqf(fmaxf(e2 - mu * mu, 0.f) + 1e-5f), of = -mu * rs;
        sfor<0, 18>([&](auto S) { u32x4 v = __builtin_bit_cast(u32x4, xf[b][S]); f32x2 t[4];
          sfor<0, 4>([&](auto E) { t[E] = (f32x2){blo(v[(int)E]), bhi(v[(int)E])}; }); SCHED();
          sfor<0, 4>([&](auto E) { t[E] = t[E] * rs + of; }); SCHED();
          sfor<0, 4>([&](auto E) { v[(int)E] = pk2(t[E].x, t[E].y); });
          pin(v); xf[b][S] = __builtin_bit_cast(bf16x8, v); SCHED(); });
        if constexpr (b == 0) { BAR(); SCHED(); } });
      STAMP(2);
    }
    f32x4 acc[9][2];
    gemm_pass<0, 9, 3>(xf, acc, smem, [](auto) {});
    { const int l = lane_id(), c = l & 15, q = l >> 4;
      STAMP(3);
      f32x2 t1[4], t2[4];
      unsigned zo = LDS_AR + w * AR + l * 16; pin(zo);
      sfor<0, 9>([&](auto F) { constexpr int f = F; u32x4 o;
        sfor<0, 4>([&](auto E) { constexpr int e = E; const f32x2 x = {acc[f][e >> 1][2 * (e & 1)], acc[f][e >> 1][2 * (e & 1) + 1]};
          const f32x2 y = gelu2(x);
          if constexpr (f == 0) { t1[e] = y; t2[e] = y * y; } else { t1[e] += y; t2[e] += y * y; }
          o[e] = pk2(y.x, y.y); });
        *(LAS u32x4*)(smw + zo + f * 1024) = o; SCHED(); });
      float y[8];
      sfor<0, 8>([&](auto E) { y[E] = t1[E >> 1][E & 1]; });
      asm volatile("s_nop 1");
      sfor<0, 8>([&](auto E) { dpp_fold<0>(y[E], t2[E >> 1][E & 1]); });
      sfor<0, 4>([&](auto E) { dpp_fold<1>(y[E], y[E + 4]); });
      asm volatile("s_nop 1");
      sfor<0, 4>([&](auto E) { y[E] = dpp_add<0x4e>(dpp_add<0xb1>(y[E])); });
      const float ya = (c & 1) ? y[1] : y[0], yb = (c & 1) ? y[3] : y[2];
      st8(rw, WS_STAT + ((i * 16 + j) * 128 + w * 32 + q * 8 + (c & 7)) * 16 + (c & 8), (u32x2){__float_as_uint((c & 2) ? yb : ya), (c & 8) ? (unsigned)(tag >> 32) : (unsigned)tag});
      LGKM0();
      STAMP(4);
    }
    gemm_pass<1, 7, 0>(xf, acc, smem, [](auto) {});
    { const int l = lane_id(), c = l & 15, q = l >> 4;
      STAMP(5);
      const unsigned so = (c >> 3) * 2048 + (w * 32 + q * 8 + (c & 7)) * 16;
      u32x4 gr[8];
      auto gather = [&]() { sfor<0, 8>([&](auto E) { gr[E] = ld16s(rw, so, WS_STAT + (i * 16 + 2 * E) * 2048); }); SCHED(); };
      gather();
      u32x2 pq[7][2];
      sfor<0, 14>([&](auto I) { constexpr int f = I >> 1, b = I & 1; pq[f][b] = (u32x2){pk2(acc[f][b][0], acc[f][b][1]), pk2(acc[f][b][2], acc[f][b][3])}; pin(pq[f][b]); });
      SCHED();
      unsigned zo = LDS_AR + w * AR + l * 16; pin(zo);
      u32x4 zr[9];
      sfor<0, 9>([&](auto F) { zr[F] = *(const LAS u32x4*)(smem + zo + F * 1024); });
      unsigned ro = LDS_AR + w * AR + c * 80 + q * 8, r6 = LDS_AR + w * AR + 64 + c * 80 + (q >> 1) * AR_V + (q & 1) * ((q >> 1) ? 8 : AR_K);
      pin(ro); pin(r6);
      asm volatile("" ::: "memory");
      sfor<0, 2>([&](auto B) { constexpr int b = B;
        sfor<0, 6>([&](auto F) { constexpr int f = (F + 2) % 6; *(LAS u32x2*)(smw + ro + ((f >> 1) == 0 ? AR_Q : ((f >> 1) == 1 ? AR_K : AR_V)) + b * 1280 + (f & 1) * 32) = pq[f][b]; });
        *(LAS u32x2*)(smw + r6 + b * 1280) = (u32x2){q == 3 ? 0x3f80u : pq[6][b].x, q == 3 ? 0u : pq[6][b].y}; });
      SCHED(); LGKM0(); BAR(); SCHED();
      float s1, s2;
#pragma nounroll
      for (int it = 0; it < SPIN_CAP; ++it) {
        unsigned bad = 0;
        sfor<0, 8>([&](auto E) { bad |= (gr[E].y ^ (unsigned)tag) | (gr[E].w ^ (unsigned)(tag >> 32));
          if constexpr (E == 0) { s1 = __uint_as_float(gr[0].x); s2 = __uint_as_float(gr[0].z); } else { s1 += __uint_as_float(gr[E].x); s2 += __uint_as_float(gr[E].z); } });
        if (__all(bad == 0)) break;
        __builtin_amdgcn_s_sleep(4);
        gather();
      }
      s1 = dpp_add<0x128>(s1); s2 = dpp_add<0x128>(s2);
      const float mu = s1 * (1.f / 2304.f);
      const float scl = __builtin_amdgcn_rsqf(fmaxf(s2 * (1.f / 2304.f) - mu * mu, 0.f) + 1e-5f), ofl = -mu * scl;
      f32x2 sc[4], of[4];
      sfor<0, 8>([&](auto E) { sc[E >> 1][E & 1] = __int_as_float(__builtin_amdgcn_update_dpp(0, __float_as_int(scl), 0x150 + E, 0xf, 0xf, true));
        of[E >> 1][E & 1] = __int_as_float(__builtin_amdgcn_update_dpp(0, __float_as_int(ofl), 0x150 + E, 0xf, 0xf, true)); });
      SCHED();
      sfor<0, 9>([&](auto F) { constexpr int f = F; f32x2 t[4]; u32x4 o;
        sfor<0, 4>([&](auto E) { t[E] = (f32x2){blo(zr[f][(int)E]), bhi(zr[f][(int)E])}; }); SCHED();
        sfor<0, 4>([&](auto E) { t[E] = t[E] * sc[E] + of[E]; }); SCHED();
        sfor<0, 4>([&](auto E) { o[(int)E] = pk2(t[E].x, t[E].y); });
        __builtin_amdgcn_raw_buffer_store_b128(o, rw, l * 16, WS_Z2 + (j * 64 + 4 * i + w) * 9216 + f * 1024, 16);
        SCHED(); asm volatile("s_nop 1"); SCHED(); });
      LGKM0();
      STAMP(6);
    }
    { int pend = 1;
      const char* fzp = ws + WS_FZ2 + ((j * 16 + i) * 4 + w) * 8;
      gemm_pass<2, 9, 3>(xf, acc, smem, [&](auto KT) { if constexpr (KT >= 3 && KT <= 7) flag_when_drained<KT == 7>(pend, fzp, tag); }); }
    const int l = lane_id(), c = l & 15, q = l >> 4;
    STAMP(7);
    u32x2 z1[9][2];
    unsigned bias[2];
    sfor<0, 2>([&](auto B) { bias[B] = P.bsp[i * 128 + w * 32 + 8 * (c >> 2) + 4 * B + (c & 3)]; });
    SCHED();
    sfor<0, 18>([&](auto I) { constexpr int f = I >> 1, b = I & 1;
      const f32x2 y0 = gelu2((f32x2){acc[f][b][0], acc[f][b][1]}), y1 = gelu2((f32x2){acc[f][b][2], acc[f][b][3]});
      z1[f][b] = (u32x2){pk2(y0.x, y0.y), pk2(y1.x, y1.y)}; pin(z1[f][b]); if constexpr (b) SCHED(); });
    sfor<0, 18>([&](auto I) { constexpr int f = I >> 1, b = I & 1; const float bb = blo(bias[b]); acc[f][b] = (f32x4){bb, bb, bb, bb}; });
    STAMP(8);
    {
      unsigned ao[4][2], bo[4];
      sfor<0, 4>([&](auto U) { ao[U][0] = U * SL2 + (w * 32 + c) * 128 + ((q ^ ((c >> 1) & 7)) << 4); ao[U][1] = ao[U][0] ^ 64; bo[U] = U * SL2 + 16384 + l * 16;
        pin(ao[U][0]); pin(ao[U][1]); pin(bo[U]); });
      bf16x8 fa0[2], fb0[9], fa1[2], fb1[9];
      auto rd = [&](bf16x8(&fa)[2], bf16x8(&fb)[9], auto U, auto KS) { constexpr int u = U, ks = KS;
        fa[0] = lds_b128(smem + ao[u][ks]); fa[1] = lds_b128(smem + ao[u][ks] + 2048);
        sfor<0, 9>([&](auto F) { fb[F] = lds_b128(smem + bo[u] + (ks * 9 + F) * 1024); }); };
      const std::integral_constant<int, 0> I0; const std::integral_constant<int, 1> I1;
      auto mm = [&](bf16x8(&fa)[2], bf16x8(&fb)[9]) {
        sfor<0, 9>([&](auto F) { constexpr int f = F, b0 = f & 1, b1 = b0 ^ 1;
          acc[f][b0] = MFMA16(fb[f], fa[b0], acc[f][b0]); acc[f][b1] = MFMA16(fb[f], fa[b1], acc[f][b1]); });
        sfor<0, 11>([&](auto) { __builtin_amdgcn_sched_group_barrier(0x8, 1, 0); __builtin_amdgcn_sched_group_barrier(0x100, 1, 0); });
        __builtin_amdgcn_sched_group_barrier(0x8, 7, 0); SCHED(); };
      SCHED(); BAR(); SCHED();
      STAMP(9);
      rd(fa0, fb0, I0, I0);
      for (int tt = 0; tt < 8; ++tt) {
        sfor<0, 4>([&](auto U) { constexpr int u = U;
          rd(fa1, fb1, U, I1); mm(fa0, fb0);
          LGKM0(); BAR(); SCHED();
          rd(fa0, fb0, std::integral_constant<int, (u + 1) & 3>{}, I0); mm(fa1, fb1); });
      }
    }
    STAMP(10);
    {
      auto up = [&](auto F, auto B) { constexpr int f = F, b = B;
        return (u32x2){pk2(acc[f][b][0] * blo(z1[f][b].x), acc[f][b][1] * bhi(z1[f][b].x)), pk2(acc[f][b][2] * blo(z1[f][b].y), acc[f][b][3] * bhi(z1[f][b].y))}; };
      const unsigned ub = WS_U + ((i * 4 + w) * 16 + j) * 9216;
      sfor<0, 8>([&](auto I) { constexpr int b = I >> 2, a = I & 3;
        const u32x2 x0 = up(std::integral_constant<int, 2 * a>{}, std::integral_constant<int, b>{}), x1 = up(std::integral_constant<int, 2 * a + 1>{}, std::integral_constant<int, b>{});
        __builtin_amdgcn_raw_buffer_store_b128((u32x4){x0.x, x0.y, x1.x, x1.y}, rw, l * 16, ub + I * 1024, 16); SCHED(); asm volatile("s_nop 1"); SCHED(); });
      const u32x2 x0 = up(std::integral_constant<int, 8>{}, std::integral_constant<int, 0>{}), x1 = up(std::integral_constant<int, 8>{}, std::integral_constant<int, 1>{});
      __builtin_amdgcn_raw_buffer_store_b64(x0, rw, l * 8, ub + 8192, 16); __builtin_amdgcn_raw_buffer_store_b64(x1, rw, l * 8, ub + 8704, 16);
      SCHED(); BAR(); SCHED();
    }
    f32x4 o3[5][4];
    auto half = [&](auto ST, const u32x4& wh) { const bool on = (c >> 3) == ST; u32x4 r = {on ? wh.x : 0u, on ? wh.y : 0u, on ? wh.z : 0u, on ? wh.w : 0u}; pin(r); return r; };
    auto step = [&](auto ST, auto FIRST, const u32x4* wv, const u32x4& wh, const u32x4(&uf)[4]) {
      const u32x4 wm = half(ST, wh);
      sfor<0, 4>([&](auto T) { const bf16x8 u = __builtin_bit_cast(bf16x8, uf[T]);
        sfor<0, 4>([&](auto Bk) { mfma_t<FIRST>(o3[Bk][T], __builtin_bit_cast(bf16x8, wv[Bk]), u); });
        mfma_t<FIRST>(o3[4][T], __builtin_bit_cast(bf16x8, wm), u); }); };
    const std::true_type YES; const std::false_type NO;
    const std::integral_constant<int, 0> J0; const std::integral_constant<int, 1> J1;
    const unsigned vl = l * 16;
    u32x4 wr[2][9];
    auto ldw = [&](auto R, int pair) { const unsigned so = WS_W3 + (cg * 45 + pair) * 9216;
      sfor<0, 9>([&](auto I) { wr[R][I] = ld16s(rw, vl, so + I * 1024); }); };
    {
      u32x4 uf[4][4];
      auto ldo = [&](auto Pr) { constexpr int pr = Pr; ldw(Pr, 36 + w + 4 * pr);
        sfor<0, 8>([&](auto I) { uf[2 * pr + (I >> 2)][I & 3] = ld16s(rw, vl, WS_OG + ((8 * i3 + 4 * th + (((I & 3) + w) & 3)) * 16 + 2 * (w + 4 * pr) + (I >> 2)) * 1024); }); SCHED(); };
      ldo(J0);
      VMW(17);
      if (l == 0) st_flag(ws, WS_FU + ((i * 16 + j) * 4 + w) * 8, tag);
      SCHED();
      STAMP(11);
      ldo(J1);
      step(J0, YES, &wr[0][0], wr[0][8], uf[0]); step(J1, NO, &wr[0][4], wr[0][8], uf[1]);
      SCHED();
      ldw(J0, 9 * w + off3);
      SCHED();
      step(J0, NO, &wr[1][0], wr[1][8], uf[2]); step(J1, NO, &wr[1][4], wr[1][8], uf[3]);
      SCHED();
      ldw(J1, 9 * w + ((off3 + 1) & 7));
    }
    unsigned go;
    u32x4 xr0, xr1; u32x2 xr2;
    {
      constexpr int UD = 3, XS = 19 - UD;
      u32x4 uf[UD][4];
      u32x2 uh[2][4][2];
      const u32x4 rq = {(unsigned)(size_t)ws, (unsigned)((size_t)ws >> 32) & 0xffffu, WS_END, 0x00020000u};
      const unsigned vh = vl >> 1;
      unsigned tb[4];
      sfor<0, 4>([&](auto T) { const int t = (T + w) & 3; tb[T] = WS_U + ((i3 * 4 + 2 * th + (t >> 1)) * 16 + 4 * w) * 9216 + (t & 1) * 4096; });
      auto ldu = [&](auto S, auto T) { constexpr int s = S, t = T;
        if constexpr (s >= 16) {
          const unsigned o = tb[t] + 2 * (s - 16) * 9216 + 8192 - (((t + w) & 1) ? 4096 - 512 : 0);
          ld8_asm(uh[s - 16][t][0], rq, vh, o); ld8_asm(uh[s - 16][t][1], rq, vh, o + 9216);
        } else { const int p = (off3 + (s >> 1)) & 7; ld16_asm<(s < UD), (s & 1) * 1024>(uf[s % UD][t], rq, vl, tb[t] + (p >> 1) * 9216 + (p & 1) * 2048); }
        SCHED(); };
      u32x2 ol[4];
      sfor<0, 4>([&](auto I) { ol[I] = ld8(rw, WS_OL + (8 * (I >> 1) + 2 * q + (I & 1)) * 16384 + (128 * i3 + 64 * th + 16 * w + c) * 8); });
      sfor<0, 18>([&](auto I) { pin(wr[I / 9][I % 9]); });
      sfor<0, 4>([&](auto I) { pin(ol[I]); });
      STAMP(12);
      SCHED(); BAR(); SCHED();
      STAMP(13);
      sfor<0, 3>([&](auto S) { sfor<0, 4>([&](auto T) { ldu(S, T); }); });
      unsigned a8 = LDS_W8 + vl; pin(a8);
      const u32x4 w88 = *(const LAS u32x4*)(smem + a8 + 8192);
      sfor<0, 2>([&](auto S) { u32x4 w8[4];
        sfor<0, 4>([&](auto I) { w8[I] = *(const LAS u32x4*)(smem + a8 + (4 * S + I) * 1024); });
        u32x4 uu = {ol[2 * S].x, ol[2 * S].y, ol[2 * S + 1].x, ol[2 * S + 1].y}; pin(uu);
        const u32x4 wm = half(S, w88);
        asm volatile("s_nop 1");
        sfor<0, 4>([&](auto Bk) { mfma_t<false>(o3[Bk][0], __builtin_bit_cast(bf16x8, w8[Bk]), __builtin_bit_cast(bf16x8, uu)); });
        mfma_t<false>(o3[4][0], __builtin_bit_cast(bf16x8, wm), __builtin_bit_cast(bf16x8, uu)); SCHED(); });
      sfor<3, UD>([&](auto S) { sfor<0, 4>([&](auto T) { ldu(S, T); }); });
      unsigned wo[2] = {(unsigned)(w * 9216) + vl, (unsigned)(w * 9216 + 2 * SL3) + vl};
      pin(wo[0]); pin(wo[1]);
      sfor<0, 18>([&](auto S) { constexpr int s = S, r = s >> 1, st = s & 1;
        const u32x4 wm = half(std::integral_constant<int, st>{}, wr[r & 1][8]);
        SCHED();
        sfor<0, 4>([&](auto T) { constexpr int t = T;
          constexpr int id = 4 * s + t, last = id + 4 * UD - 1 < 71 ? id + 4 * UD - 1 : 71;
          constexpr int cnt = (last - id) + (last >= 64 ? last - (id >= 64 ? id : 63) : 0) + (s >= XS ? 3 : 0);
          VMW(cnt);
          u32x4 uv;
          if constexpr (s < 16) uv = uf[s % UD][t];
          else { pin(uh[s - 16][t][0]); pin(uh[s - 16][t][1]); uv = (u32x4){uh[s - 16][t][0].x, uh[s - 16][t][0].y, uh[s - 16][t][1].x, uh[s - 16][t][1].y}; pin(uv); asm volatile("s_nop 1"); }
          const bf16x8 u = __builtin_bit_cast(bf16x8, uv);
          sfor<0, 4>([&](auto Bk) { mfma_t<false>(o3[Bk][t], __builtin_bit_cast(bf16x8, wr[r & 1][4 * st + Bk]), u); });
          mfma_t<false>(o3[4][t], __builtin_bit_cast(bf16x8, wm), u);
          SCHED();
          if constexpr (s + UD < 18) ldu(std::integral_constant<int, s + UD>{}, T); });
        if constexpr (s + 1 == XS) { const int l2 = lane_id(), c2 = l2 & 15;
          go = ((i3 * 128 + th * 64 + (w >> 1) * 32 + 8 * (c2 >> 2) + 4 * (w & 1) + (c2 & 3)) * 576 + cg * 72) * 2 + (l2 & 48);
          const char* xp = (const char*)P.x + go;
          xr0 = *(const u32x4*)xp; xr1 = *(const u32x4*)(xp + 64); xr2 = *(const u32x2*)(xp + 128 - (l2 & 48) + ((l2 >> 1) & 8)); SCHED(); }
        if constexpr (st == 1 && r + 2 < 9) {
          constexpr int sl = r & 3;
          if constexpr (r > 0) { LGKM0(); BAR(); SCHED(); }
          sfor<0, 9>([&](auto I) { wr[r & 1][I] = *(const LAS u32x4*)(smem + wo[sl >> 1] + (sl & 1) * SL3 + I * 1024); });
          SCHED(); } });
    }
    STAMP(14);
    asm volatile("s_nop 7" ::: "memory");
    SCHED();
    sfor<1, 4>([&](auto T) { LAS char* rp = smw + ((((T + w) & 3) * 3 + (T - 1)) * 5) * 1024 + vl;
      sfor<0, 5>([&](auto Bk) { *(LAS f32x4*)(rp + Bk * 1024) = o3[Bk][T]; }); });
    f32x4 r[5];
    r[0] = o3[0][0] + (f32x4){blo(xr0.x), bhi(xr0.x), blo(xr0.y), bhi(xr0.y)}; r[1] = o3[1][0] + (f32x4){blo(xr0.z), bhi(xr0.z), blo(xr0.w), bhi(xr0.w)};
    r[2] = o3[2][0] + (f32x4){blo(xr1.x), bhi(xr1.x), blo(xr1.y), bhi(xr1.y)}; r[3] = o3[3][0] + (f32x4){blo(xr1.z), bhi(xr1.z), blo(xr1.w), bhi(xr1.w)};
    r[4] = o3[4][0];
    SCHED(); LGKM0(); BAR(); SCHED();
    f32x4 pr[15];
    sfor<0, 15>([&](auto I) { pr[I] = *(const LAS f32x4*)(smem + (w * 15 + (I % 3) * 5 + I / 3) * 1024 + vl); });
    SCHED();
    sfor<0, 15>([&](auto I) { r[I / 3] += pr[I]; });
    char* op = (char*)P.out + go;
    *(u32x4*)op = (u32x4){pk2(r[0][0], r[0][1]), pk2(r[0][2], r[0][3]), pk2(r[1][0], r[1][1]), pk2(r[1][2], r[1][3])};
    *(u32x4*)(op + 64) = (u32x4){pk2(r[2][0], r[2][1]), pk2(r[2][2], r[2][3]), pk2(r[3][0], r[3][1]), pk2(r[3][2], r[3][3])};
    float hs[4];
    sfor<0, 4>([&](auto E) { const unsigned v = __float_as_uint(r[4][(int)E]); auto a = __builtin_amdgcn_permlane32_swap(v, v, false, false); hs[E] = __uint_as_float(a[0]) + __uint_as_float(a[1]); });
    if (vl < 512) *(u32x2*)(op + 128 - ((vl >> 5) & 8)) = (u32x2){pk2(hs[0] + blo(xr2.x), hs[1] + bhi(xr2.x)), pk2(hs[2] + blo(xr2.y), hs[3] + bhi(xr2.y))};
    STAMP(15);
    ST_FLUSH();
  } else {
    const int m = w - 5, hw = w - 4;
    int nb = 0, lim = 0;
    if (m == 2 && rr < CODE_KB / 8) touch((const char*)pc0 + rr * 8192, l * 128, lds0 + LDS_KVB);
    auto tick = [&]() { SCHED(); nb = __builtin_amdgcn_readfirstlane(nb); asm volatile("s_cmp_ge_i32 %0, %1\n\ts_cbranch_scc1 1f\n\ts_barrier\n\ts_add_i32 %0, %0, 1\n1:" : "+s"(nb) : "s"(lim) : "scc", "memory"); SCHED(); };
    auto upto = [&](int n) { SCHED(); nb = __builtin_amdgcn_readfirstlane(nb); asm volatile("0:\n\ts_cmp_ge_i32 %0, %1\n\ts_cbranch_scc1 1f\n\ts_barrier\n\ts_add_i32 %0, %0, 1\n\ts_branch 0b\n1:" : "+s"(nb) : "s"(n) : "scc", "memory"); SCHED(); };
    const u64 tag = get_tag();
    auto vmtick = [&]() { SCHED(); nb = __builtin_amdgcn_readfirstlane(nb); TR_ARR(); unsigned st;
      asm volatile("0:\n\ts_getreg_b32 %1, hwreg(HW_REG_IB_STS)\n\ts_and_b32 %1, %1, 0xc000000f\n\ts_cbranch_scc0 1f\n\ts_cmp_ge_i32 %0, %2\n\ts_cbranch_scc1 1f\n\t"
                   "s_barrier\n\ts_add_i32 %0, %0, 1\n\ts_branch 0b\n1:\n\ts_waitcnt vmcnt(0)" : "+s"(nb), "=&s"(st) : "s"(lim) : "scc", "memory"); SCHED(); };
    auto wait_tick = [&](unsigned off) {
#pragma nounroll
      for (int it = 0; it < SPIN_CAP; ++it) { const u64 f = ld_flag(ws, off); vmtick(); if (__all(f == tag)) break; tick(); __builtin_amdgcn_s_sleep(1); }
      asm volatile("" ::: "memory"); };
    const int t32 = l & 31, h = l >> 5;
    unsigned vw[18], vq[18];
    auto tile_offsets = [&](auto QKV, unsigned(&v)[18]) {
      int pos = (l * 57) >> 10, m = l - pos * 18;
      sfor<0, 9>([&](auto Pp) { constexpr int p = Pp;
        const int kr = (pos & 3) | ((pos & 12) << 1) | ((pos >> 2) & 4);
        unsigned o;
        if constexpr (QKV) { const int m2 = m > 14 ? 14 : m, sec = (m2 * 13) >> 6, mm = m2 - sec * 5; o = kr * 3456 + sec * 1152 + (mm < 4 ? mm * 16 : 56); }
        else { const int m2 = m + 2 * (j & 1); o = kr * 9216 + (m2 >= 18 ? m2 - 18 : m2) * 16; }
        v[p] = o + 4096 - (p & 3) * 1024; v[p + 9] = o + 32 * (QKV ? 3456 : 9216) + 4096 - ((p + 9) & 3) * 1024;
        pin(v[p]); pin(v[p + 9]);
        m += 10; pos += 3; if (m >= 18) { m -= 18; ++pos; } }); };
    const int Pq = bid >> 2, Qr = bid & 3;
    unsigned v3[6], a3[6];
    if (w == 4) { tile_offsets(std::false_type{}, vw); tile_offsets(std::true_type{}, vq); }
    else {
      const int wq = Pq / 9, pp = Pq - 9 * wq, jp = 4 * wq + (pp >> 1), kind = Pq < 36 ? (pp == 8) : (Pq < 44 ? 2 : 3);
      const int rb = kind == 0 ? 144 * jp + 16 * (jp & 1) + 64 * (pp & 1) : (kind == 1 ? 576 * wq + 128 : (kind == 2 ? 72 * (Pq - 36) : 16));
      const int c1 = kind == 3 ? 18 : 2, c2 = kind < 2 ? 4 : (kind == 2 ? 8 : 72), c3 = kind < 2 ? 8 : (kind == 2 ? 18 : 144), c4 = kind < 2 ? 16 : (kind == 2 ? 4 : 36),
                c5 = kind == 0 ? 32 : (kind == 2 ? 36 : 288);
      const int n0 = m * 384 + l; int pos = (n0 * 3641) >> 16, mm = n0 - pos * 18;
      const int p = l & 3; const unsigned ab = LDS_AR + (l >> 2) * 288;
      sfor<0, 6>([&](auto Pp) {
        v3[Pp] = (rb + (pos & 1) + c1 * ((pos >> 1) & 1) + c2 * ((pos >> 2) & 1) + c3 * ((pos >> 3) & 1) + c4 * ((pos >> 4) & 1) + c5 * (pos >> 5)) * 1152 + mm * 16; pin(v3[Pp]);
        mm += 10; pos += 3; if (mm >= 18) { mm -= 18; ++pos; }
        const int fi = m * 6 + Pp, cgp = fi >= 9, x = fi - 9 * cgp;
        a3[Pp] = ab + 144 * cgp + (x < 8 ? (x >> 2) * 9216 + (x & 2) * 32 + (x & 1) * 8 + 16 * p : (p >> 1) * 9216 + 128 + 8 * (p & 1)); pin(a3[Pp]); });
    }
    bf16x8 wf[2][3];
    { const int hp = (t32 >> 2) & 1, reg = (t32 & 3) + 4 * (t32 >> 3);
      const int d0 = 8 * h - 18 * hp;
      const LAS u16* wp = (const LAS u16*)(smem + LDS_WPHI + hw * 1536) + 8 * h * 18 + reg;
      VMW(0);
      sfor<0, 6>([&](auto I) { constexpr int bk = I / 3, s = I % 3; u32x4 v;
        sfor<0, 4>([&](auto E) { constexpr int e = E;
          const unsigned x0 = wp[(16 * s + 2 * e) * 18 + 16 * bk], x1 = wp[(16 * s + 2 * e + 1) * 18 + 16 * bk];
          const bool ok = (unsigned)(d0 + 16 * s + 2 * e) < 18u && (bk == 0 || reg < 2);
          v[e] = ok ? (x0 | (x1 << 16)) : 0u; });
        pin(v); wf[bk][s] = __builtin_bit_cast(bf16x8, v); }); }
    const char* wz1 = (const char*)P.win + j * 288 - 4096;
    const char* wq = (const char*)P.wqkv + j * 72 - 4096;
    auto issue_n = [&](auto N) { constexpr int n = N;
      if constexpr (n < 9) issue_tile<18>(wz1 + 4608 + ktile(n) * (64 * 9216), vw, lds0 + ((n + 3) % 6) * SL1);
      else if constexpr (n < 18) issue_tile<18>(wq + ktile(n - 9) * (64 * 3456), vq, lds0 + ((n + 3) % 6) * SL1);
      else if constexpr (n < 27) issue_tile<18>(wz1 + ktile(n - 18) * (64 * 9216), vw, lds0 + ((n + 3) % 6) * SL1); };
    auto pass_step = [&](auto M, auto ENT) { constexpr int m = M, ent = ENT;
      constexpr int hi0 = (m + 3 > ent ? m + 3 : ent), hi = hi0 > 26 ? 26 : hi0;
      VMW((hi - m) * 18);
      TBAR();
      if constexpr (m + 4 > ent) issue_n(std::integral_constant<int, m + 4>{}); };
    unsigned va[16];
    if (w == 4) {
      sfor<0, 3>(issue_n);
      TBAR();
      sfor<3, 4>(issue_n);
      sfor<0, 9>([&](auto M) { pass_step(M, std::integral_constant<int, 3>{}); });
      TBAR();
      sfor<13, 15>(issue_n);
      sfor<0, 16>([&](auto Pp) { constexpr int p = Pp; const int row = p * 8 + (l >> 3), cc = row & 15;
        const int tok = (row & ~31) + 8 * (cc >> 2) + 4 * ((row >> 4) & 1) + (cc & 3);
        va[p] = tok * 4096 + (((l & 7) ^ ((cc >> 1) & 7)) << 4) + 4096 - (p & 3) * 1024; pin(va[p]); });
      sfor<9, 17>([&](auto M) { pass_step(M, std::integral_constant<int, 14>{}); });
      VMW(54); TBAR(); TBAR();
      __builtin_amdgcn_s_setprio(1);
      TBAR();
    } else {
    __builtin_amdgcn_s_setprio(1);
    upto(NB_XDONE);
    if (bid < 180) { const char* src = (const char*)(Pq < 36 ? P.wout : P.wo) + Qr * 288;
      sfor<0, 6>([&](auto Pp) { glds_m0<0, 0>(src, v3[Pp], lds0 + LDS_AR + (m * 6 + Pp) * 1024); }); }
    lim = NB_A0 + 4;
    vmtick();
    upto(NB_A0 + 5);
    bf16x8 w3f[6];
    sfor<0, 6>([&](auto Pp) { w3f[Pp] = lds_tr(smem + a3[Pp], smem + a3[Pp] + 4608); });
    SCHED();
    sfor<0, 6>([&](auto Pp) { pin(w3f[Pp]); });
    STAMP(1);
    upto(NB_ADONE);
    if (bid < 180) sfor<0, 6>([&](auto Pp) { const int fi = m * 6 + Pp, cgp = fi >= 9, x = fi - 9 * cgp;
      __builtin_amdgcn_raw_buffer_store_b128(__builtin_bit_cast(u32x4, w3f[Pp]), rw, l * 16, WS_W3 + ((2 * Qr + cgp) * 45 + Pq) * 9216 + x * 1024, 16); });
    if (m == 0) {
      const char* wb = (const char*)P.wsp + bid * 32768;
      sfor<0, 4>([&](auto I) { touch(wb, l * 128 + I * 8192, lds0 + LDS_SCR); });
    }
      lim = NB_BDONE;
      vmtick();
      upto(NB_BDONE + 1);
    }
    const unsigned opo = t32 * 80 + h * 16, opo2 = t32 * 80 + 64;
    bf16x8 qf[2][3];
    const int g0 = m + (m >> 1);
    { const LAS char* kp = smem + LDS_AR + hw * AR + AR_K;
      const bf16x8 kf[3] = {lds_b128(kp + opo), lds_b128(kp + opo + 32), lds_b128(kp + opo2)};
      sfor<0, 2>([&](auto U) { const LAS char* qp = smem + LDS_AR + ((g0 + U) & 3) * AR + AR_Q;
        qf[U][0] = lds_b128(qp + opo); qf[U][1] = lds_b128(qp + opo + 32); qf[U][2] = lds_b128(qp + opo2); });
      u32x4 ph[5];
      hedgehog<true>(wf, kf, ph, []() {});
      sfor<0, 6>([&](auto I) { pin(qf[I / 3][I % 3]); });
      sfor<0, 5>([&](auto S) { *(LAS u32x4*)(smw + LDS_AR + hw * AR + AR_P + t32 * 160 + h * 80 + S * 16) = ph[S]; });
      LGKM0(); }
    STAMP(2);
    if (w == 4) {
      __builtin_amdgcn_s_setprio(0);
      VMW(36); TBAR();
      sfor<21, 23>(issue_n);
      sfor<19, 24>([&](auto M) { pass_step(M, std::integral_constant<int, 22>{}); });
      const unsigned vl = l * 16;
      const char* ab = (const char*)P.wsp + (size_t)i * 128 * 4096 - 4096;
      const char* zb = ws + WS_Z2 + j * (64 * 9216);
      const int ph = (i + j) & 3;
      auto issue_g2 = [&](int s) { const int t = ((s & 3) == ph) ? s : ((s - 4) & 31); const unsigned sl = lds0 + (s & 3) * SL2;
        issue_tile<16>(ab + t * 128, va, sl); issue_lin<18>(zb + t * 18432, vl, sl + 16384); };
      const unsigned fz = WS_FZ2 + j * 512 + l * 8;
      VMW(36); TBAR();
      const u64 f0 = ld_flag(ws, fz);
      VMW(19); TBAR();
      const bool early = __all(f0 == tag);
      VMW(0);
      if (early) issue_g2(0);
      TBAR();
      if (early) issue_g2(1);
      TBAR();
      STAMP(1);
      if (!early) { wait_flags(ws, fz, tag); issue_g2(0); issue_g2(1); }
      if (bid == 0 && l == 0) st_flag(ws, WS_EPOCH, tag);
      issue_g2(2);
      VMW(63); TBAR();
      issue_g2(3); VMW(63); TBAR();
#pragma nounroll
      for (int t = 2; t < 30; ++t) { issue_g2(t + 2); VMW(63); TBAR(); }
      STAMP(3);
      const char* w3 = ws + WS_W3 + cg * (45 * 9216);
      auto issue_g3 = [&](int r) { const unsigned sl = lds0 + ((r - 2) & 3) * SL3; const int p = r == 8 ? 8 : ((off3 + r) & 7);
        sfor<0, 4>([&](auto W) { constexpr int ww = W; issue_lin<9>(w3 + (ww * 9 + p) * 9216, vl, sl + ww * 9216); }); };
      VMW(34); TBAR();
      issue_g3(2);
      VMW(36); TBAR();
      TBAR();
      TBAR();
      issue_lin<9>(w3 + 44 * 9216, vl, lds0 + LDS_W8);
#define SEAMV 1
#if SEAMV == 0
      issue_g3(3);
      VMW(36); TBAR();
      issue_g3(4); issue_g3(5); VMW(63);
#elif SEAMV == 1
      issue_g3(3); issue_g3(4);
      VMW(63); TBAR();
      issue_g3(5); VMW(63);
#else
      issue_g3(3); issue_g3(4); issue_g3(5);
      VMW(63); TBAR();
#endif
#pragma nounroll
      for (int r = 6; r < 9; ++r) { TBAR(); issue_g3(r); VMW(63); }
      TBAR();
      VMW(36); TBAR();
      VMW(0); TBAR();
      STAMP(4);
      TR_FLUSH(); ST_FLUSH();
    } else {
      upto(NB_C0);
      lim = NB_CDONE;
      if (m < 2) {
        const int p = l & 3;
        const unsigned pa = LDS_AR + AR_P + (l >> 2) * 160 + m * 80 + 8 * p, pa2 = pa + 64 - 8 * (p & 2);
        const unsigned pv = LDS_AR + AR_V + (l >> 2) * 80 + m * 32 + 8 * p, pv2 = LDS_AR + AR_V + (l >> 2) * 80 + (p ? 72 : m * 32 + 32);
        f32x4 kv[3][2];
        sfor<0, 4>([&](auto G) { constexpr int g = G;
          const bf16x8 a0 = lds_tr(smem + pa + g * AR, smem + pa + g * AR + 2560), a1 = lds_tr(smem + pa + g * AR + 32, smem + pa + g * AR + 2592),
                       a2 = lds_tr(smem + pa2 + g * AR, smem + pa2 + g * AR + 2560);
          const bf16x8 b0 = lds_tr(smem + pv + g * AR, smem + pv + g * AR + 1280), b1 = lds_tr(smem + pv2 + g * AR, smem + pv2 + g * AR + 1280);
          const f32x4 z = {0.f, 0.f, 0.f, 0.f};
          kv[0][0] = MFMA16(a0, b0, g ? kv[0][0] : z); kv[0][1] = MFMA16(a0, b1, g ? kv[0][1] : z);
          kv[1][0] = MFMA16(a1, b0, g ? kv[1][0] : z); kv[1][1] = MFMA16(a1, b1, g ? kv[1][1] : z);
          kv[2][0] = MFMA16(a2, b0, g ? kv[2][0] : z); kv[2][1] = MFMA16(a2, b1, g ? kv[2][1] : z);
          if constexpr (g & 1) { sfor<0, 6>([&](auto I) { pin(kv[I >> 1][I & 1]); }); tick(); } });
        const unsigned o0 = (q >> 1) * 768 + c * 32 + (q & 1) * 16, o1 = c < 8 ? o0 + 512 : 0x7ffffff0u;
        sfor<0, 6>([&](auto I) { constexpr int fb = I >> 1, db = I & 1; const unsigned o = db ? o1 : o0;
          __builtin_amdgcn_raw_buffer_store_b128(__builtin_bit_cast(u32x4, kv[fb][db]), rw, fb == 2 && q >= 2 ? 0x7ffffff0u : o, WS_KVP + (j * 16 + i) * 8192 + (m * 5 + 2 * fb) * 768, 16); });
        vmtick();
        if (l == 0) st_flag(ws, WS_FKVP + ((j * 16 + i) * 2 + m) * 8, tag);
        STAMP(3);
      }
      lim = NB_KV - 1;
      if (m == 2) {
        upto(NB_CDONE - 2);
        wait_tick(WS_FKVP + (j * 32 + t32) * 8);
        STAMP(3);
        const unsigned o = i < 15 ? (i * 32 + t32) * 16 + h * (8 * 8192) : 0x7ffffff0u;
        u32x4 t[8];
        sfor<0, 8>([&](auto I) { t[I] = ld16s(rw, o, WS_KVP + (j * 16 + I) * 8192); });
        if (nb != NB_CDONE) vmtick();
        float s[4];
        sfor<0, 4>([&](auto E) { s[E] = __uint_as_float(t[0][(int)E]); sfor<1, 8>([&](auto I) { s[E] += __uint_as_float(t[I][(int)E]); }); pin(s[E]);
          auto a = __builtin_amdgcn_permlane32_swap(__float_as_uint(s[E]), __float_as_uint(s[E]), false, false); s[E] = __uint_as_float(a[0]) + __uint_as_float(a[1]); });
        st8(rw, h == 0 ? WS_KVB + j * 4096 + (i * 32 + t32) * 8 : 0x7ffffff0u, (u32x2){pk2s(s[0], s[1]), pk2s(s[2], s[3])});
        vmtick();
        if (l == 0) st_flag(ws, WS_FKVB + (j * 16 + i) * 8, tag);
        STAMP(4);
      }
      upto(NB_G2);
      u32x4 ph[2][5];
      hedgehog<false>(wf, qf[0], ph[0], tick); tick();
      if (m == 1) { hedgehog<false>(wf, qf[1], ph[1], tick); tick(); }
      if (m == 0) {
        upto(NB_KV - 6);
        wait_tick(WS_FKVB + (j * 16 + c) * 8);
        issue_lin<4>(ws + WS_KVB + j * 4096, l * 16, lds0 + LDS_KVB);
        vmtick();
      }
      STAMP(5);
      __builtin_amdgcn_s_setprio(1);
      upto(NB_KV);
      lim = NB_G2DONE - 3;
      u32x4 kf[2][5];
      { const int hp = (t32 >> 2) & 1, reg = (t32 & 3) + 4 * (t32 >> 3);
        sfor<0, 2>([&](auto Bk) { constexpr int bk = Bk; const bool ok = hp == h && (bk == 0 || reg < 3);
          const unsigned o = LDS_KVB + (ok ? (h * 120 + (bk && reg == 2 ? 20 : 16 * bk + reg + 2 * h)) * 16 : 3840), st = ok ? 384 : 0;
          sfor<0, 5>([&](auto S) { kf[bk][S] = *(const LAS u32x4*)(smem + o + st * S); }); }); }
      SCHED();
      sfor<0, 10>([&](auto I) { pin(kf[I / 5][I % 5]); });
      tick();
      const f32x16 z16 = {0.f, 0.f, 0.f, 0.f, 0.f, 0.f, 0.f, 0.f, 0.f, 0.f, 0.f, 0.f, 0.f, 0.f, 0.f, 0.f};
      auto unit = [&](auto U) { const int g = g0 + U;
        f32x16 n0 = z16, n1 = z16;
        sfor<0, 5>([&](auto S) { n0 = MFMA32(__builtin_bit_cast(bf16x8, kf[0][S]), __builtin_bit_cast(bf16x8, ph[U][S]), n0);
          n1 = MFMA32(__builtin_bit_cast(bf16x8, kf[1][S]), __builtin_bit_cast(bf16x8, ph[U][S]), n1); if constexpr (S & 1) { pin(n0); pin(n1); tick(); } });
        const float r = 1.0f / (n1[2] + 1e-6f);
        u32x4 o0, o1;
        sfor<0, 4>([&](auto E) { o0[(int)E] = pk2s(n0[2 * E] * r, n0[2 * E + 1] * r); o1[(int)E] = pk2s(n0[8 + 2 * E] * r, n0[9 + 2 * E] * r); });
        const unsigned ob = WS_OG + ((8 * i + 2 * g + (t32 >> 4)) * 16 + j) * 1024 + (32 * h + c) * 16;
        st16(rw, ob, o0); st16(rw, ob + 256, o1);
        st4(rw, WS_OL + j * 16384 + (128 * i + 32 * g + t32) * 8 + h * 4, pk2s(n1[0] * r, n1[1] * r));
        tick(); };
      unit(std::integral_constant<int, 0>{});
      if (m == 1) unit(std::integral_constant<int, 1>{});
      vmtick();
      if (l == 0) st_flag(ws, WS_FO + (i * 16 + j) * 32 + m * 8, tag);
      STAMP(6);
      if (m == 0) {
        upto(NB_G2DONE - 6);
        wait_tick(WS_FO + i3 * 512 + ((l & 3) == 3 ? l - 1 : l) * 8);
        upto(NB_S0 - 1);
        __builtin_amdgcn_s_sleep(U_POLL_DELAY);
        wait_flags(ws, WS_FU + ((i3 * 16 + (t32 >> 1)) * 4 + 2 * th + (l & 1)) * 8, tag);
      }
      STAMP(7);
      upto(NB_LAST);
      TR_FLUSH(); ST_FLUSH();
    }
  }
#endif
}

extern "C" void kernel_launch(void* const* d_in, const int* in_sizes, int n_in, void* d_out, int out_size, void* d_ws, size_t ws_size,
                              hipStream_t stream) {
  Params p;
  p.x = (const u16*)d_in[0]; p.wqkv = (const u16*)d_in[1]; p.wphi = (const u16*)d_in[2]; p.wo = (const u16*)d_in[3];
  p.win = (const u16*)d_in[5]; p.wsp = (const u16*)d_in[6]; p.bsp = (const u16*)d_in[7]; p.wout = (const u16*)d_in[8];
  p.out = (u16*)d_out; p.ws = (char*)d_ws;
  (void)hipFuncSetAttribute((const void*)fused_block, hipFuncAttributeMaxDynamicSharedMemorySize, LDS_TOTAL);
  fused_block<<<256, 512, LDS_TOTAL, stream>>>(p);
}
```
